```python
import torch

def _softmax(x, axis=-1):
    return torch.softmax(x, dim=axis)
def _gelu(x):
    return torch.nn.functional.gelu(x, approximate="tanh")
def _silu(x):
    return torch.nn.functional.silu(x)
def _sigmoid(x):
    return torch.sigmoid(x)
def _layernorm(x, axis=-1, eps=1e-5):
    m = x.mean(dim=axis, keepdim=True)
    v = x.var(dim=axis, keepdim=True, unbiased=False)
    return (x - m) / torch.sqrt(v + eps)
def _scatter_add(tgt, idx, src, axis=0):
    return tgt.clone().index_add_(axis, idx, src)
def _segment_sum(src, seg, n):
    out = torch.zeros((n,) + src.shape[1:], dtype=src.dtype)
    return out.index_add_(0, seg, src)
def _pad(x, pw):
    flat = tuple(v for pair in reversed(pw) for v in pair)
    return torch.nn.functional.pad(x, flat)

kernel_name = 'gated_mix__sliding_local_dilated__glu_gated_glu__prompt'

def setup_inputs():

    _w = (lambda a: (lambda t: t.to(torch.bfloat16) if t.is_floating_point() else t)(torch.as_tensor(a)))
    def _A_setup():
        B, S, D = 1, 2048, 576
        return {
            "x": _w((lambda *s: torch.randn(*s, dtype=torch.bfloat16) * 0.05)(B, S, D)),
            "wqkv": _w((lambda *s: torch.randn(*s, dtype=torch.bfloat16) * 0.8)(D, 3 * D)),
            "wo": _w((lambda *s: torch.randn(*s, dtype=torch.bfloat16) * 0.05)(D, D)),
        }
    _A_in = _A_setup()

    def _B_setup():
        B, S, D, F = 1, 2048, 576, 4608
        return {
            "x": _w((lambda *s: torch.randn(*s, dtype=torch.bfloat16) * 0.05)(B, S, D)),
            "w_gate": _w((lambda *s: torch.randn(*s, dtype=torch.bfloat16) * 1.6)(D, F)),
            "w_up": _w((lambda *s: torch.randn(*s, dtype=torch.bfloat16) * 0.1)(D, F)),
            "w_down": _w((lambda *s: torch.randn(*s, dtype=torch.bfloat16) * 0.1)(F, D)),
        }
    _B_in = _B_setup()

    return {
        "A_x": _A_in["x"],
        "A_wqkv": _A_in["wqkv"],
        "A_wo": _A_in["wo"],
        "B_x": _B_in["x"],
        "B_w_gate": _B_in["w_gate"],
        "B_w_up": _B_in["w_up"],
        "B_w_down": _B_in["w_down"],
        "gate_w": _w((lambda *s: torch.randn(*s, dtype=torch.bfloat16) * 0.05)(576) * 16.0),
    }

def reference(A_x, A_wqkv, A_wo, B_x, B_w_gate, B_w_up, B_w_down, gate_w):
    def _A_ref(x, wqkv, wo):
        B, S, D = x.shape
        H = 32; Dh = D // H
        W, dil = 8, 2
        qkv = (lambda x, *d: x.permute(*d))((x @ wqkv).reshape(B, S, 3, H, Dh), 2, 0, 3, 1, 4)
        q, k, v = qkv[0], qkv[1], qkv[2]
        scale = Dh ** -0.5
        pos = torch.arange(S)
        off = torch.arange(W) * dil
        idx = pos[:, None] - off[None, :]
        mask = idx >= 0
        idx_c = torch.where(mask, idx, 0).reshape(-1)
        kg = k[:, :, idx_c, :].reshape(B, H, S, W, Dh)
        vg = v[:, :, idx_c, :].reshape(B, H, S, W, Dh)
        logits = torch.einsum("bhsd,bhswd->bhsw", q, kg) * scale
        logits = torch.where(mask[None, None, :, :], logits, -1e9)
        attn = _softmax(logits, axis=-1)
        o = torch.einsum("bhsw,bhswd->bhsd", attn, vg)
        return (lambda x, *d: x.permute(*d))(o, 0, 2, 1, 3).reshape(B, S, D) @ wo

    def _B_ref(x, w_gate, w_up, w_down):
        g = torch.einsum("bsd,df->bsf", x, w_gate)
        u = torch.einsum("bsd,df->bsf", x, w_up)
        h = _sigmoid(g) * u
        return torch.einsum("bsf,fd->bsd", h, w_down)

    x = A_x
    g = _sigmoid((x * gate_w).sum(-1, keepdims=True))
    a = _A_ref(x, A_wqkv, A_wo)
    b = _B_ref(x, B_w_gate, B_w_up, B_w_down)
    return g * a + (1 - g) * b
```

```cpp
#include <hip/hip_runtime.h>
#include <hip/hip_bf16.h>
typedef unsigned short u16;
__device__ __forceinline__ float bf2f(u16 v) { return __uint_as_float(((unsigned)v) << 16); }
__device__ __forceinline__ u16 f2bf(float f) { unsigned u = __float_as_uint(f); u += 0x7fffu + ((u >> 16) & 1u); return (u16)(u >> 16); }
__device__ __forceinline__ float rbf(float f) { return bf2f(f2bf(f)); }
constexpr int S = 2048, D = 576, F = 4608, H = 32, DH = 18, W = 8, DIL = 2;

__global__ void n_gemm(const u16* A, const u16* B, u16* C, int M, int N, int K) {
  __shared__ float As[16][17], Bs[16][17];
  int tx = threadIdx.x, ty = threadIdx.y;
  int row = blockIdx.y * 16 + ty, col = blockIdx.x * 16 + tx;
  float acc = 0.f;
  for (int k0 = 0; k0 < K; k0 += 16) {
    As[ty][tx] = bf2f(A[(size_t)row * K + k0 + tx]);
    Bs[ty][tx] = bf2f(B[(size_t)(k0 + ty) * N + col]);
    __syncthreads();
    for (int k = 0; k < 16; ++k) acc += As[ty][k] * Bs[k][tx];
    __syncthreads();
  }
  C[(size_t)row * N + col] = f2bf(acc);
}
__global__ void n_attn(const u16* qkv, u16* o) {
  int idx = blockIdx.x * blockDim.x + threadIdx.x;
  if (idx >= S * H) return;
  int s = idx / H, h = idx % H;
  const float scale = rbf(0.23570226039551584f);
  float lg[W];
  float mx = -3e38f;
  for (int w = 0; w < W; ++w) {
    int p = s - w * DIL;
    float l;
    if (p >= 0) {
      float d = 0.f;
      for (int j = 0; j < DH; ++j) d += bf2f(qkv[(size_t)s * 3 * D + h * DH + j]) * bf2f(qkv[(size_t)p * 3 * D + D + h * DH + j]);
      l = rbf(rbf(d) * 0.23570226039551584f);
    } else l = rbf(-1e9f);
    lg[w] = l; mx = fmaxf(mx, l);
  }
  float sum = 0.f;
  for (int w = 0; w < W; ++w) { lg[w] = expf(lg[w] - mx); sum += lg[w]; }
  for (int w = 0; w < W; ++w) lg[w] = rbf(lg[w] / sum);
  for (int j = 0; j < DH; ++j) {
    float a = 0.f;
    for (int w = 0; w < W; ++w) { int p = s - w * DIL; if (p < 0) p = 0; a += lg[w] * bf2f(qkv[(size_t)p * 3 * D + 2 * D + h * DH + j]); }
    o[(size_t)s * D + h * DH + j] = f2bf(a);
  }
  (void)scale;
}
__global__ void n_act(const u16* g, const u16* u, u16* h, int n) {
  int i = blockIdx.x * blockDim.x + threadIdx.x;
  if (i >= n) return;
  float gg = bf2f(g[i]);
  float sg = rbf(1.f / (1.f + expf(-gg)));
  h[i] = f2bf(sg * bf2f(u[i]));
}
__global__ void n_final(const u16* x, const u16* gw, const u16* a, const u16* b, u16* out) {
  int s = blockIdx.x;
  __shared__ float red[256];
  float p = 0.f;
  for (int d = threadIdx.x; d < D; d += 256) p += rbf(bf2f(x[s * D + d]) * bf2f(gw[d]));
  red[threadIdx.x] = p; __syncthreads();
  for (int o = 128; o > 0; o >>= 1) { if (threadIdx.x < o) red[threadIdx.x] += red[threadIdx.x + o]; __syncthreads(); }
  float t = rbf(red[0]);
  float g = rbf(1.f / (1.f + expf(-t)));
  float omg = rbf(1.f - g);
  for (int d = threadIdx.x; d < D; d += 256)
    out[s * D + d] = f2bf(rbf(g * bf2f(a[s * D + d])) + rbf(omg * bf2f(b[s * D + d])));
}

static void naive_launch(void* const* d_in, void* d_out, void* d_ws, hipStream_t stream) {
  const u16* x = (const u16*)d_in[0]; const u16* wqkv = (const u16*)d_in[1]; const u16* wo = (const u16*)d_in[2];
  const u16* wg = (const u16*)d_in[4]; const u16* wu = (const u16*)d_in[5]; const u16* wd = (const u16*)d_in[6];
  const u16* gw = (const u16*)d_in[7];
  u16* ws = (u16*)d_ws;
  u16* qkv = ws; u16* o = qkv + (size_t)S * 3 * D; u16* a = o + (size_t)S * D; u16* g = a + (size_t)S * D;
  u16* u = g + (size_t)S * F; u16* h = u + (size_t)S * F; u16* b = h + (size_t)S * F;
  dim3 blk(16, 16);
  n_gemm<<<dim3(3 * D / 16, S / 16), blk, 0, stream>>>(x, wqkv, qkv, S, 3 * D, D);
  n_attn<<<S * H / 256, 256, 0, stream>>>(qkv, o);
  n_gemm<<<dim3(D / 16, S / 16), blk, 0, stream>>>(o, wo, a, S, D, D);
  n_gemm<<<dim3(F / 16, S / 16), blk, 0, stream>>>(x, wg, g, S, F, D);
  n_gemm<<<dim3(F / 16, S / 16), blk, 0, stream>>>(x, wu, u, S, F, D);
  n_act<<<S * F / 256, 256, 0, stream>>>(g, u, h, S * F);
  n_gemm<<<dim3(D / 16, S / 16), blk, 0, stream>>>(h, wd, b, S, D, F);
  n_final<<<S, 256, 0, stream>>>(x, gw, a, b, (u16*)d_out);
}
extern "C" void kernel_launch(void* const* d_in, const int* in_sizes, int n_in, void* d_out, int out_size, void* d_ws, size_t ws_size, hipStream_t stream) {
  naive_launch(d_in, d_out, d_ws, stream);
}
```

```cpp
#include <hip/hip_runtime.h>
#include <hip/hip_bf16.h>
#define PRIO
#include <stdint.h>
#include <utility>
template <int N> struct IC { static constexpr int v = N; };
template <class F, int... Is> __device__ __forceinline__ void sfor_(F&& f, std::integer_sequence<int, Is...>) { (f(IC<Is>{}), ...); }
template <int N, class F> __device__ __forceinline__ void sfor(F&& f) { sfor_(f, std::make_integer_sequence<int, N>{}); }

typedef __bf16 bf16x2 __attribute__((ext_vector_type(2)));
typedef __bf16 bf16x4 __attribute__((ext_vector_type(4)));
typedef __bf16 bf16x8 __attribute__((ext_vector_type(8)));
typedef float f32x2 __attribute__((ext_vector_type(2)));
typedef float f32x4 __attribute__((ext_vector_type(4)));
typedef unsigned u32x2 __attribute__((ext_vector_type(2)));
typedef unsigned u32x4 __attribute__((ext_vector_type(4)));
#define LAS __attribute__((address_space(3)))
#define DI __device__ __forceinline__
#define SB() __builtin_amdgcn_sched_barrier(0)
#define MFMA16(a, b, c) __builtin_amdgcn_mfma_f32_16x16x32_bf16((a), (b), (c), 0, 0, 0)

constexpr int S_ = 2048, D_ = 576, F_ = 4608;
constexpr int NJ = 162;
constexpr int WSLOT = 24576, OFF_G = 0, OFF_U = 9216, OFF_Q = 18432, OFF_H = 23552, XRING = 98304, XSLOT = 32768, LDS_K1 = 163840;
constexpr int RSW = 288, RSQ = 160;
constexpr float LOG2E = 1.4426950408889634f;

DI unsigned pk2(float lo, float hi) { f32x2 v = {lo, hi}; bf16x2 b = __builtin_convertvector(v, bf16x2); return __builtin_bit_cast(unsigned, b); }
DI bf16x4 trhalf(unsigned addr, int off) { return __builtin_amdgcn_ds_read_tr16_b64_v4bf16((LAS bf16x4*)(addr + off)); }
DI bf16x8 trfrag(unsigned addr, int off0, int off1) { return __builtin_shufflevector(trhalf(addr, off0), trhalf(addr, off1), 0, 1, 2, 3, 4, 5, 6, 7); }
DI bf16x8 ld128(unsigned addr, int off) { return *(LAS bf16x8*)(addr + off); }
DI void dma_s(unsigned ldsdst, unsigned voff, const void* sbase) {
  asm volatile("s_mov_b32 m0, %0\n\ts_nop 0\n\tglobal_load_lds_dwordx4 %1, %2" ::"s"(ldsdst), "v"(voff), "s"(sbase));
}
DI void dma_v(unsigned ldsdst, const void* vaddr) {
  asm volatile("s_mov_b32 m0, %0\n\ts_nop 0\n\tglobal_load_lds_dwordx4 %1, off" ::"s"(ldsdst), "v"(vaddr));
}
#define WAITV(n) asm volatile("s_waitcnt vmcnt(" #n ")" ::: "memory")
#define BAR() do { asm volatile("" ::: "memory"); __builtin_amdgcn_s_barrier(); asm volatile("" ::: "memory"); } while (0)
#define STAMP(buf, i)
DI int kap(int rho) { return 8 * ((rho >> 2) & 3) + 4 * (rho >> 4) + (rho & 3); }
DI int phi(int t) { return (-t) & 3; }
DI float red_max(float v) {
  auto a = __builtin_amdgcn_permlane16_swap(__float_as_uint(v), __float_as_uint(v), false, false);
  v = fmaxf(__uint_as_float(a[0]), __uint_as_float(a[1]));
  auto b = __builtin_amdgcn_permlane32_swap(__float_as_uint(v), __float_as_uint(v), false, false);
  return fmaxf(__uint_as_float(b[0]), __uint_as_float(b[1]));
}
DI float red_sum(float v) {
  auto a = __builtin_amdgcn_permlane16_swap(__float_as_uint(v), __float_as_uint(v), false, false);
  v = __uint_as_float(a[0]) + __uint_as_float(a[1]);
  auto b = __builtin_amdgcn_permlane32_swap(__float_as_uint(v), __float_as_uint(v), false, false);
  return __uint_as_float(b[0]) + __uint_as_float(b[1]);
}

struct K1Dma {
  const char* sx; const char* sg; const char* su; const char* v4;
  unsigned vx, vw, st4, l4, lx, lw;
  DI void wpiece(int i, unsigned slotbase) {
    if (i == 0) { dma_s(slotbase + OFF_G + lw, vw, sg); sg += 32 * 9216; }
    if (i == 1) { dma_s(slotbase + OFF_U + lw, vw, su); su += 32 * 9216; }
    if (i == 2) { dma_v(slotbase + l4, v4); v4 += st4; }
  }
  DI void xpiece(int i, unsigned slotbase) { dma_s(slotbase + lx + 1024 * i, vx, sx + 8 * 1152 * i); if (i == 3) sx += 128; }
};

#define PD 2
template <bool ISB>
DI void k1_main(f32x4 (&acc)[11][4], f32x4& acch, K1Dma& dm, unsigned lbase, unsigned ax0, unsigned ag, unsigned ae0, unsigned ae1, unsigned ae2, unsigned axh, int lane) {
  constexpr int NT = ISB ? 12 : 11;
  auto whalf = [&](int j, int h, unsigned so) -> bf16x4 {
    if (j < 8) return trhalf(ag + so, (j & 1) * 9216 + (j >> 1) * 32 + h * 16 * RSW);
    if (!ISB) {
      if (j == 8) return trhalf(ae0 + so, h * 16 * RSW);
      if (j == 9) return trhalf(ae0 + so, 9216 + h * 16 * RSW);
      return trhalf(ae1 + so, h * 16 * RSQ);
    } else {
      if (j == 8) return trhalf(ae0 + so, h * 16 * RSQ);
      if (j == 9) return trhalf(ae0 + so, 48 + h * 16 * RSQ);
      if (j == 10) return trhalf(ae1 + so, h * 16 * RSQ);
      return trhalf(ae2 + so, h * 16 * RSQ);
    }
  };
  const bool row0 = (lane & 15) == 0;
  bf16x8 xf[4], xh;
  bf16x4 wc[PD][2];
  unsigned so = 0, xs = 0;
#pragma unroll
  for (int t = 0; t < 4; ++t) xf[t] = ld128(ax0, t * 2048);
#pragma unroll
  for (int j = 0; j < PD; ++j) { wc[j][0] = whalf(j, 0, so); wc[j][1] = whalf(j, 1, so); }
#pragma clang loop unroll(disable)
  for (int dd = 0; dd < 9; ++dd) {
    int d = dd; asm volatile("" : "+s"(d));
#pragma unroll
    for (int par = 0; par < 2; ++par) {
      const int s = 2 * d + par;
      const unsigned sn = so == 3 * WSLOT ? 0 : so + WSLOT;
      const unsigned sp = so == 0 ? 3 * WSLOT : so - WSLOT;
      const unsigned axn = par == 0 ? (ax0 ^ 64u) + xs : ax0 + (xs ^ XSLOT);
      bf16x8 xn[4];
      bf16x4 w[NT + PD][2];
#pragma unroll
      for (int j = 0; j < PD; ++j) { w[j][0] = wc[j][0]; w[j][1] = wc[j][1]; }
#pragma unroll
      for (int j = 0; j < NT; ++j) {
        if (j == 4) {
          SB();
          if (par == 0) { if (d < 8) WAITV(7); else WAITV(0); }
          else { if (d < 8) WAITV(3); else WAITV(0); }
          BAR();
          SB();
        }
        bf16x8 a = __builtin_shufflevector(w[j][0], w[j][1], 0, 1, 2, 3, 4, 5, 6, 7);
        if (ISB && j == 10) {
          u32x4 av = __builtin_bit_cast(u32x4, a), hv = __builtin_bit_cast(u32x4, xh);
#pragma unroll
          for (int e = 0; e < 4; ++e) av[e] = row0 ? hv[e] : av[e];
          a = __builtin_bit_cast(bf16x8, av);
        }
        const int jn = j + PD;
#pragma unroll
        for (int t = 0; t < 4; ++t) {
          if (j < 11) acc[j][t] = MFMA16(a, xf[t], acc[j][t]);
          else if (t == 0) acch = MFMA16(a, xh, acch);
          SB();
          if (t < 2) w[jn][t] = jn < NT ? whalf(jn, t, so) : whalf(jn - NT, t, sn);
          if (t == 2 && j >= 5 && j < 9) xn[j - 5] = ld128(axn, (j - 5) * 2048);
          if (t == 2 && ISB && j == 2) xh = ld128(axh + so, 0);
          if (t == 3 && j >= 4 && j < 7) { if (s + 3 < 18) dm.wpiece(j - 4, lbase + sp); }
          if (par == 1 && t == 3 && j >= 7 && j < 11) { if (d < 7) dm.xpiece(j - 7, lbase + XRING + xs); }
          SB();
        }
      }
#pragma unroll
      for (int t = 0; t < 4; ++t) xf[t] = xn[t];
#pragma unroll
      for (int j = 0; j < PD; ++j) { wc[j][0] = w[NT + j][0]; wc[j][1] = w[NT + j][1]; }
      so = sn;
    }
    xs ^= XSLOT;
  }
}

constexpr int AQ = 0, AK = 17408, AV = 34816, RSV = 96, AG = AV + 272 * RSV;

__global__ __launch_bounds__(512, 2) void k1(const char* __restrict__ x, const char* __restrict__ wqkv, const char* __restrict__ wg,
                                              const char* __restrict__ wu, const char* __restrict__ gw, char* __restrict__ hf) {
  extern __shared__ __attribute__((aligned(16))) char smem[];
  const int tid = threadIdx.x; int lane = tid & 63;
  const int wave = __builtin_amdgcn_readfirstlane(tid >> 6);
  const int bid = blockIdx.x, xcd = bid & 7, idx = bid >> 3;
  const int m = 4 * (xcd & 1) + (idx & 3), c = 8 * (xcd >> 1) + (idx >> 2);
  const int wm = wave & 3; const bool isB = wave >= 4;
  const unsigned lbase = (unsigned)(size_t)smem;
  int tau = lane & 15, g = lane >> 4, q4 = (lane & 15) >> 2, p4 = lane & 3;
  unsigned long long* sb = (unsigned long long*)(hf + (128u << 20));
  STAMP(sb, 0);
  if (bid < 64 && tid == 0) *(unsigned*)(hf + (64u << 20) + bid * 256) = 0u;
  K1Dma dm;
  {
    dm.vx = (lane >> 3) * 1152 + (((lane & 7) ^ (lane >> 3)) << 4);
    dm.sx = x + (size_t)(256 * m + 32 * wave) * 1152;
    dm.lx = 4096 * wave;
    const int b = 1024 * wave + 16 * lane, rho = b / RSW, cc = (b % RSW) >> 4;
    dm.vw = kap(rho) * 9216 + cc * 16;
    dm.sg = wg + 288 * c; dm.su = wu + 288 * c;
    dm.lw = 1024 * wave;
    dm.wpiece(0, lbase); dm.wpiece(1, lbase);
#pragma unroll
    for (int i = 0; i < 4; ++i) dm.xpiece(i, lbase + XRING);
    if (wave == 0) {
      const int r = lane >> 2, ch = (lane & 3) ^ phi(r >> 2);
      int tok = 256 * m - 16 + r; if (tok < 0) tok = 0;
      dm.v4 = (r == 0 ? gw : x + (size_t)tok * 1152) + ch * 16;
      dm.st4 = 64; dm.l4 = OFF_H;
    } else if (wave <= 2) {
      const int b8 = 8192 + 16 * lane, rho8 = b8 / RSW, cc8 = (b8 % RSW) >> 4;
      dm.v4 = (wave == 1 ? wg : wu) + 288 * c + kap(rho8) * 9216 + cc8 * 16;
      dm.st4 = 32 * 9216; dm.l4 = (wave == 1 ? OFF_G : OFF_U) + 8192;
    } else {
      const int bq = 1024 * (wave - 3) + 16 * lane, rq = bq / RSQ; int cq = (bq % RSQ) >> 4; if (cq == 9) cq = 8;
      const int seg = cq / 3, sub = cq - 3 * seg;
      dm.v4 = wqkv + kap(rq) * 3456 + 1152 * seg + 36 * c + (sub < 2 ? 16 * sub : 20);
      dm.st4 = 32 * 3456; dm.l4 = OFF_Q + 1024 * (wave - 3);
    }
    dm.wpiece(2, lbase);
#pragma unroll
    for (int i = 0; i < 3; ++i) dm.wpiece(i, lbase + WSLOT);
#pragma unroll
    for (int i = 0; i < 3; ++i) dm.wpiece(i, lbase + 2 * WSLOT);
#pragma unroll
    for (int i = 0; i < 4; ++i) dm.xpiece(i, lbase + XRING + XSLOT);
  }

  f32x4 acc[11][4]; f32x4 acch = {0.f, 0.f, 0.f, 0.f};
#pragma unroll
  for (int j = 0; j < 11; ++j)
#pragma unroll
    for (int t = 0; t < 4; ++t) acc[j][t] = (f32x4){0.f, 0.f, 0.f, 0.f};

  unsigned xoff = tau * 64 + ((g ^ phi(tau >> 2)) << 4);
  const unsigned ax0 = lbase + XRING + 8192 * wm + tau * 128 + ((g ^ (tau & 7)) << 4), axh = lbase + OFF_H + xoff;
  const int odd = c & 1;
  const unsigned trw = (4 * g + q4) * RSW + 8 * p4, trq = (4 * g + q4) * RSQ;
  const unsigned t3col = p4 == 0 ? 40 : 40 + 48 * (p4 - 1);
  WAITV(10); BAR();
  if (isB) __builtin_amdgcn_s_setprio(1);
  STAMP(sb, 1);
  if (!isB) {
    k1_main<false>(acc, acch, dm, lbase, ax0, lbase + OFF_G + trw + 32 * odd, lbase + OFF_G + trw + (odd ? 0 : 256), lbase + OFF_Q + trq + 8 * p4, 0, 0, lane);
  } else {
    const unsigned hsel = wm == 0 ? 48 + 8 * p4 : (wm == 1 ? 96 + 8 * p4 : t3col);
    k1_main<true>(acc, acch, dm, lbase, ax0, lbase + OFF_G + trw + 32 * (4 + odd), lbase + OFF_Q + trq + 48 + 8 * p4, lbase + OFF_Q + trq + t3col, lbase + OFF_Q + trq + hsel, axh, lane);
  }
  asm volatile("s_waitcnt lgkmcnt(0)" ::: "memory");
  BAR();
  STAMP(sb, 2);
  lane = __builtin_amdgcn_mbcnt_hi(~0u, __builtin_amdgcn_mbcnt_lo(~0u, 0u));
  asm volatile("" : "+v"(lane));
  tau = lane & 15; g = lane >> 4; q4 = (lane & 15) >> 2; p4 = lane & 3;
  xoff = tau * 64 + ((g ^ phi(tau >> 2)) << 4);
  {
    const unsigned qk8 = tau * 64 + ((((g >> 1) ^ phi(tau >> 2))) << 4) + (g & 1) * 8;
    auto st8 = [&](unsigned a, f32x4 v) { u32x2 w = {pk2(v[0], v[1]), pk2(v[2], v[3])}; *(LAS u32x2*)(a) = w; };
    if (!isB) {
#pragma unroll
      for (int t = 0; t < 4; ++t) st8(lbase + AQ + (1 + 4 * wm + t) * 1024 + qk8, acc[10][t]);
    } else {
      const unsigned c2 = tau * 64 + ((2 ^ phi(tau >> 2)) << 4);
      const unsigned b3 = g == 0 ? AG - 512 + tau * 32 : (g == 3 ? AV + tau * RSV + 32 : (g == 1 ? AQ : AK) + c2);
      const unsigned s3 = g == 0 ? 512 : (g == 3 ? 16 * RSV : 1024);
      auto st3 = [&](unsigned a1, f32x4 v) {
        u32x4 w1 = {g == 0 ? __float_as_uint(v[0]) : pk2(v[2], v[3]), 0u, 0u, 0u}, z = {0u, 0u, 0u, 0u};
        *(LAS u32x4*)(a1) = w1; *(LAS u32x4*)(a1 ^ 16u) = z;
      };
#pragma unroll
      for (int t = 0; t < 4; ++t) {
        const int tile = 1 + 4 * wm + t;
        st8(lbase + AK + tile * 1024 + qk8, acc[8][t]);
        st8(lbase + AV + (tile * 16 + tau) * RSV + 8 * g, acc[9][t]);
        st3(lbase + b3 + tile * s3, acc[10][t]);
      }
      if (wm == 0) st8(lbase + AK + qk8, acch);
      else if (wm == 1) st8(lbase + AV + tau * RSV + 8 * g, acch);
      else if (wm == 2) st3(lbase + (g == 0 ? AG + 8192 + tau * 32 : b3), acch);
    }
  }
  asm volatile("s_waitcnt lgkmcnt(0)" ::: "memory");
  BAR();
  STAMP(sb, 3);
  bf16x8 qf[2], kf[3], vf[2][2]; float zq[2], zc[4];
#pragma unroll
  for (int t = 0; t < 4; ++t) zc[t] = *(LAS float*)(lbase + AG + (64 * wm + 16 * t + tau) * 32);
#pragma unroll
  for (int i = 0; i < 3; ++i) kf[i] = ld128(lbase + AK + 2 * wave * 1024 + xoff, i * 1024);
#pragma unroll
  for (int qi = 0; qi < 2; ++qi) {
    const int qt = 2 * wave + qi;
    qf[qi] = ld128(lbase + AQ + (qt + 1) * 1024 + xoff, 0);
    const unsigned va = lbase + AV + (qt * 16 + 4 * g + q4) * RSV + 8 * p4;
    vf[qi][0] = trfrag(va, 0, 16 * RSV); vf[qi][1] = trfrag(va, 32, 32 + 16 * RSV);
    zq[qi] = *(LAS float*)(lbase + AG + (16 * qt + tau) * 32);
  }
  char* hb = hf + (size_t)((4 * m + wm) * NJ + 9 * (c >> 1)) * 4096 + lane * 16;
  const int j0 = isB ? (odd ? 7 : 2) : (odd ? 5 : 0);
  auto epi = [&](int t) {
    const float cc = fminf(1.f + __builtin_amdgcn_exp2f(zc[t] * LOG2E), 3.0e38f);
    auto hv = [&](int jg) -> u32x2 {
      u32x2 o;
#pragma unroll
      for (int r = 0; r < 2; ++r) {
        f32x2 gv = {acc[jg][t][2 * r], acc[jg][t][2 * r + 1]}, uv = {acc[jg + 1][t][2 * r], acc[jg + 1][t][2 * r + 1]};
        gv = gv * -LOG2E;
        f32x2 e = {__builtin_amdgcn_exp2f(gv[0]), __builtin_amdgcn_exp2f(gv[1])};
        e = e * cc + cc;
        f32x2 rr = {__builtin_amdgcn_rcpf(e[0]), __builtin_amdgcn_rcpf(e[1])};
        uv = uv * rr;
        o[r] = pk2(uv[0], uv[1]);
      }
      return o;
    };
#pragma unroll
    for (int jj = 0; jj < 2; ++jj) {
      u32x2 lo = hv(4 * jj), hi = hv(4 * jj + 2);
      *(u32x4*)(hb + (j0 + jj) * 4096 + t * 1024) = (u32x4){lo[0], lo[1], hi[0], hi[1]};
    }
    if (!isB) { u32x2 sv = hv(8); *(u32x2*)(hb + 4 * 4096 + t * 1024 + 8 * odd) = sv; }
  };
  const f32x4 zero = {0.f, 0.f, 0.f, 0.f};
  epi(0);
  f32x4 sc[2][2];
#pragma unroll
  for (int qi = 0; qi < 2; ++qi) { sc[qi][0] = MFMA16(kf[qi], qf[qi], zero); sc[qi][1] = MFMA16(kf[qi + 1], qf[qi], zero); }
  epi(1);
  f32x4 ov[2][2]; float rs[2];
  {
    constexpr float SC = 0.23570226039551584f * LOG2E;
#pragma unroll
    for (int qi = 0; qi < 2; ++qi) {
      float sv[8]; float mx = -3.0e38f;
      const bool first = (m == 0) && (wave == 0) && (qi == 0);
#pragma unroll
      for (int e = 0; e < 8; ++e) {
        const int d = tau + 16 - (16 * (e >> 2) + 4 * g + (e & 3));
        const bool ok = (unsigned)d <= 14u && !(d & 1) && !(first && e < 4);
        sv[e] = ok ? sc[qi][e >> 2][e & 3] : -3.0e38f;
        mx = fmaxf(mx, sv[e]);
      }
      mx = red_max(mx);
      float sum = 0.f;
#pragma unroll
      for (int e = 0; e < 8; ++e) { sv[e] = __builtin_amdgcn_exp2f((sv[e] - mx) * SC); sum += sv[e]; }
      sum = red_sum(sum);
      rs[qi] = __builtin_amdgcn_rcpf(sum * (1.f + __builtin_amdgcn_exp2f(zq[qi] * -LOG2E)));
      u32x4 pw = {pk2(sv[0], sv[1]), pk2(sv[2], sv[3]), pk2(sv[4], sv[5]), pk2(sv[6], sv[7])};
      bf16x8 pf = __builtin_bit_cast(bf16x8, pw);
      ov[qi][0] = MFMA16(vf[qi][0], pf, zero); ov[qi][1] = MFMA16(vf[qi][1], pf, zero);
    }
  }
  epi(2);
  {
    char* ob = hf + (size_t)((4 * m + (wave >> 1)) * NJ) * 4096;
#pragma unroll
    for (int qi = 0; qi < 2; ++qi) {
      const int t4 = 2 * (wave & 1) + qi; const float gs = rs[qi];
      *(u32x2*)(ob + (144 + (c >> 1)) * 4096 + t4 * 1024 + lane * 16 + 8 * odd) = (u32x2){pk2(ov[qi][0][0] * gs, ov[qi][0][1] * gs), pk2(ov[qi][0][2] * gs, ov[qi][0][3] * gs)};
      if (g == 0) *(unsigned*)(ob + (160 + (c >> 4)) * 4096 + t4 * 1024 + (tau + 16 * ((c >> 2) & 3)) * 16 + 4 * (c & 3)) = pk2(ov[qi][1][0] * gs, ov[qi][1][1] * gs);
    }
  }
  epi(3);
  STAMP(sb, 4);
  STAMP(sb, 5);
  WAITV(0);
  STAMP(sb, 6);
}

constexpr int K2_NS = 8, K2_LD = 6, K2_CNT = 2 * K2_NS * 9216, LDS_K2 = K2_CNT + 256;
constexpr size_t WS_MAIL = 32u << 20, WS_FLAG = 64u << 20;
DI void dma_s16(unsigned ldsdst, unsigned voff, const void* sbase) {
  asm volatile("s_mov_b32 m0, %0\n\ts_mov_b64 exec, 0xffff\n\tglobal_load_lds_dwordx4 %1, %2\n\ts_mov_b64 exec, -1" ::"s"(ldsdst), "v"(voff), "s"(sbase));
}
DI void lds_arrive(unsigned addr, unsigned one) {
  asm volatile("s_mov_b64 exec, 1\n\tds_add_u32 %0, %1\n\ts_mov_b64 exec, -1" ::"v"(addr), "v"(one) : "memory");
}
template <int N> DI void waitv() { asm volatile("s_waitcnt vmcnt(%0)" ::"n"(N) : "memory"); }

__global__ __launch_bounds__(512, 2) void k2(const char* __restrict__ hf, const char* __restrict__ wd, const char* __restrict__ wo, char* __restrict__ ws, char* __restrict__ out) {
  extern __shared__ __attribute__((aligned(16))) char smem[];
  const int tid = threadIdx.x, lane = tid & 63;
  const int wave = __builtin_amdgcn_readfirstlane(tid >> 6);
  const int bid = blockIdx.x, xcd = bid & 7, idx = bid >> 3;
  const int s = xcd >> 1, mt = 8 * (xcd & 1) + (idx & 7), nt = idx >> 3;
  const int tgp = wave & 3, kh = wave >> 2;
  const int g = lane >> 4, q4 = (lane & 15) >> 2, p4 = lane & 3;
  const unsigned lbase = (unsigned)(size_t)smem;
  unsigned long long* sb = (unsigned long long*)(ws + (129u << 20));
  STAMP(sb, 0);
  if (tid < 64) *(LAS unsigned*)(lbase + K2_CNT + 4 * lane) = 0u;
  unsigned vw[3], vl[3];
#pragma unroll
  for (int i = 0; i < 3; ++i) {
    const int q = 64 * i + (i == 2 ? (lane & 15) : lane), rho = 8 * tgp + q / 18, cc = q % 18;
    vw[i] = rho * 1152 + cc * 16;
    const int e = 4 * (rho >> 4) + (rho & 3), gg = (rho >> 2) & 3;
    vl[i] = (18 * (4 * gg + (e >> 1)) + 16 + (e & 1)) * 1152 + cc * 16;
  }
  const unsigned ring = lbase + kh * (K2_NS * 9216);
  const char* swd = wd + 288 * nt + (size_t)(36 * s + kh) * (32 * 1152);
  const char* swo = wo + 288 * nt + (size_t)(36 * (4 * s + kh) + 2 * (tgp >> 1)) * 1152;
  const char* swl = wo + 288 * nt + (size_t)(18 * 16 * (s >> 1)) * 1152;
  const char* shb = hf + ((size_t)((2 * mt + (tgp >> 1)) * NJ) * 4 + 2 * (tgp & 1)) * 1024;
  const char* shf = shb + (size_t)(36 * s + kh) * 4096;
  const char* sho = shb + (size_t)(144 + 4 * s + kh) * 4096;
  const char* shl = shb + (size_t)(160 + (s >> 1)) * 4096;
  const unsigned vh = lane * 16;
  bf16x8 hr[K2_LD + 1][2];
  auto bpart = [&](int i, int part) {
    const unsigned dst = ring + (i % K2_NS) * 9216 + 2304 * tgp;
    const char* sw = i < 18 ? swd + (size_t)i * (64 * 1152) : (i < 20 ? swo + (size_t)(i - 18) * (72 * 1152) : swl);
    if (part == 0) dma_s(dst, i < 20 ? vw[0] : vl[0], sw);
    if (part == 1) dma_s(dst + 1024, i < 20 ? vw[1] : vl[1], sw);
    if (part == 2) dma_s16(dst + 2048, i < 20 ? vw[2] : vl[2], sw);
    if (part == 3) {
      const char* sh = i < 18 ? shf + (size_t)i * 8192 : (i < 20 ? sho + (size_t)(i - 18) * 8192 : shl);
      asm volatile("global_load_dwordx4 %0, %1, %2" : "=v"(hr[i % (K2_LD + 1)][0]) : "v"(vh), "s"(sh));
      asm volatile("global_load_dwordx4 %0, %1, %2 offset:1024" : "=v"(hr[i % (K2_LD + 1)][1]) : "v"(vh), "s"(sh));
    }
  };
#pragma unroll
  for (int i = 0; i < K2_LD; ++i) { bpart(i, 0); bpart(i, 1); bpart(i, 2); bpart(i, 3); }
  f32x4 acc[9][2];
#pragma unroll
  for (int j = 0; j < 9; ++j) { acc[j][0] = (f32x4){0.f, 0.f, 0.f, 0.f}; acc[j][1] = (f32x4){0.f, 0.f, 0.f, 0.f}; }
  const unsigned acnt = lbase + K2_CNT + 64 * kh, one = 1u;
  const unsigned aw = ring + (4 * g + q4) * RSW + 8 * p4;
  const unsigned aw2 = aw + 4 * 9216;
  auto whalf = [&](int i, int j, int h) -> bf16x4 {
    const int sl = i % K2_NS;
    return trhalf(sl < 4 ? aw : aw2, (sl & 3) * 9216 + 32 * j + h * 16 * RSW);
  };
  waitv<5 * (K2_LD - 1)>();
  asm volatile("s_waitcnt lgkmcnt(0)" ::: "memory");
  BAR();
  STAMP(sb, 1);
  constexpr int P2 = 3, NB = 21;
  bf16x4 wc[P2][2];
#pragma unroll
  for (int j = 0; j < P2; ++j) { wc[j][0] = whalf(0, j, 0); wc[j][1] = whalf(0, j, 1); }
  const bool has21 = kh == 0 && !(s & 1);
  auto step = [&](auto I) {
    constexpr int i = decltype(I)::v;
    unsigned pv = 0;
    bf16x4 w[9 + P2][2];
#pragma unroll
    for (int j = 0; j < P2; ++j) { w[j][0] = wc[j][0]; w[j][1] = wc[j][1]; }
    asm volatile("" : "+v"(hr[i % (K2_LD + 1)][0]), "+v"(hr[i % (K2_LD + 1)][1]));
    sfor<9>([&](auto JJ) {
      constexpr int j = decltype(JJ)::v;
      if constexpr (j == 1 && i + 1 < NB) {
        constexpr int newer = (i + K2_LD - 1 < NB ? i + K2_LD - 1 : NB - 1) - (i + 1);
        waitv<5 * newer>();
        lds_arrive(acnt, one);
      }
      if constexpr (j == 3 && i + 1 < NB) pv = *(volatile LAS unsigned*)(acnt);
      if constexpr (j == 5 && i + 1 < NB) {
        if (__builtin_expect((unsigned)__builtin_amdgcn_readfirstlane(pv) < 4u * (i + 1), 0)) {
          do { pv = *(volatile LAS unsigned*)(acnt); } while ((unsigned)__builtin_amdgcn_readfirstlane(pv) < 4u * (i + 1));
        }
        asm volatile("" ::: "memory");
      }
      if constexpr (j >= 5 && i + K2_LD < NB) bpart(i + K2_LD, j - 5);
      SB();
      constexpr int jn = j + P2;
      bf16x8 a = __builtin_shufflevector(w[j][0], w[j][1], 0, 1, 2, 3, 4, 5, 6, 7);
#pragma unroll
      for (int t = 0; t < 2; ++t) {
        acc[j][t] = MFMA16(a, hr[i % (K2_LD + 1)][t], acc[j][t]);
        SB();
        if (jn < 9) w[jn][t] = whalf(i, jn, t); else if (i + 1 < NB) w[jn][t] = whalf(i + 1, jn - 9, t);
        SB();
      }
    });
#pragma unroll
    for (int j = 0; j < P2; ++j) { wc[j][0] = w[9 + j][0]; wc[j][1] = w[9 + j][1]; }
  };
  sfor<20>(step);
  if (has21) step(IC<20>{});
  asm volatile("s_waitcnt vmcnt(0) lgkmcnt(0)" ::: "memory");
  BAR();
  STAMP(sb, 2);
  f32x4 r[9];
  {
    const unsigned xa = lbase + wave * 9216 + lane * 16, xb = lbase + (wave ^ 4) * 9216 + lane * 16;
    if (kh == 0) {
#pragma unroll
      for (int j = 0; j < 9; ++j) { *(LAS f32x4*)(xa + j * 1024) = acc[j][1]; r[j] = acc[j][0]; }
    } else {
#pragma unroll
      for (int j = 0; j < 9; ++j) { *(LAS f32x4*)(xa + j * 1024) = acc[j][0]; r[j] = acc[j][1]; }
    }
    asm volatile("s_waitcnt lgkmcnt(0)" ::: "memory");
    BAR();
#pragma unroll
    for (int j = 0; j < 9; ++j) r[j] += *(LAS f32x4*)(xb + j * 1024);
  }
  STAMP(sb, 3);
  const int grp = mt * 4 + nt;
  const unsigned FOWN = 8 * 9216;
  __amdgpu_buffer_rsrc_t mail = __builtin_amdgcn_make_buffer_rsrc(ws + WS_MAIL + (size_t)grp * (16 * 18432), 0, 16 * 18432, 0x00020000);
  if (tgp != s) {
#pragma unroll
    for (int j = 0; j < 9; ++j)
      __builtin_amdgcn_raw_buffer_store_b128(__builtin_bit_cast(u32x4, r[j]), mail, lane * 16 + j * 1024, (tgp * 4 + s) * 18432 + kh * 9216, 16);
  } else {
#pragma unroll
    for (int j = 0; j < 9; ++j) *(LAS f32x4*)(lbase + FOWN + kh * 9216 + j * 1024 + lane * 16) = r[j];
  }
  asm volatile("s_waitcnt vmcnt(0) lgkmcnt(0)" ::: "memory");
  BAR();
  STAMP(sb, 4);
  if (wave == 0) {
    unsigned* flag = (unsigned*)(ws + WS_FLAG) + grp * 64;
    if (lane == 0) __hip_atomic_fetch_add(flag, 1u, __ATOMIC_RELAXED, __HIP_MEMORY_SCOPE_AGENT);
    unsigned spins = 0;
    while ((unsigned)__builtin_amdgcn_readfirstlane(__hip_atomic_load(flag, __ATOMIC_RELAXED, __HIP_MEMORY_SCOPE_AGENT)) < 4u) {
      __builtin_amdgcn_s_sleep(1);
      if (++spins > (1u << 20)) break;
    }
  }
  BAR();
  STAMP(sb, 5);
  {
    f32x4 v[3][3];
#pragma unroll
    for (int f3 = 0; f3 < 3; ++f3) {
      const int f = wave + 8 * f3;
      if (f < 18) {
#pragma unroll
        for (int o = 0; o < 3; ++o) {
          const int src = o + (o >= s);
          v[f3][o] = __builtin_bit_cast(f32x4, __builtin_amdgcn_raw_buffer_load_b128(mail, lane * 16, (s * 4 + src) * 18432 + f * 1024, 16));
        }
      }
    }
#pragma unroll
    for (int f3 = 0; f3 < 3; ++f3) {
      const int f = wave + 8 * f3;
      if (f < 18) {
        f32x4 a = *(LAS f32x4*)(lbase + FOWN + f * 1024 + lane * 16);
        a += v[f3][0] + v[f3][1] + v[f3][2];
        const int hh = f >= 9, j = f - 9 * hh;
        const int tok = 128 * mt + 32 * s + 16 * hh + (lane & 15);
        *(u32x2*)(out + (size_t)tok * 1152 + (144 * nt + 16 * j + 4 * g) * 2) = (u32x2){pk2(a[0], a[1]), pk2(a[2], a[3])};
      }
    }
  }
  STAMP(sb, 6);
}

extern "C" void kernel_launch(void* const* d_in, const int* in_sizes, int n_in, void* d_out, int out_size, void* d_ws, size_t ws_size, hipStream_t stream) {
  const char* x = (const char*)d_in[0]; const char* wqkv = (const char*)d_in[1]; const char* wo = (const char*)d_in[2];
  const char* wg = (const char*)d_in[4]; const char* wu = (const char*)d_in[5]; const char* wd = (const char*)d_in[6];
  const char* gw = (const char*)d_in[7];
  char* hf = (char*)d_ws;
  (void)hipFuncSetAttribute((const void*)k1, hipFuncAttributeMaxDynamicSharedMemorySize, LDS_K1);
  (void)hipFuncSetAttribute((const void*)k2, hipFuncAttributeMaxDynamicSharedMemorySize, LDS_K2);
  hipLaunchKernelGGL(k1, dim3(256), dim3(512), LDS_K1, stream, x, wqkv, wg, wu, gw, hf);
  hipLaunchKernelGGL(k2, dim3(256), dim3(512), LDS_K2, stream, (const char*)hf, wd, wo, (char*)d_ws, (char*)d_out);
}
```

```cpp
#include <hip/hip_runtime.h>
#include <hip/hip_bf16.h>
#define PF0 2
#include <stdint.h>
#include <utility>
template <int N> struct IC { static constexpr int v = N; };
template <class F, int... Is> __device__ __forceinline__ void sfor_(F&& f, std::integer_sequence<int, Is...>) { (f(IC<Is>{}), ...); }
template <int N, class F> __device__ __forceinline__ void sfor(F&& f) { sfor_(f, std::make_integer_sequence<int, N>{}); }

typedef __bf16 bf16x2 __attribute__((ext_vector_type(2)));
typedef __bf16 bf16x4 __attribute__((ext_vector_type(4)));
typedef __bf16 bf16x8 __attribute__((ext_vector_type(8)));
typedef float f32x2 __attribute__((ext_vector_type(2)));
typedef float f32x4 __attribute__((ext_vector_type(4)));
typedef unsigned u32x2 __attribute__((ext_vector_type(2)));
typedef unsigned u32x4 __attribute__((ext_vector_type(4)));
typedef unsigned long long u64;
#define NTW ""
#define STP " sc1"
#define LAS __attribute__((address_space(3)))
#define DI __device__ __forceinline__
#define SB() __builtin_amdgcn_sched_barrier(0)
#define MFMA16(a, b, c) __builtin_amdgcn_mfma_f32_16x16x32_bf16((a), (b), (c), 0, 0, 0)
#define BAR() do { asm volatile("" ::: "memory"); __builtin_amdgcn_s_barrier(); asm volatile("" ::: "memory"); } while (0)
#define PIN(x) asm volatile("" : "+v"(x))
#define STAMP(i)

constexpr int NJ = 162;
constexpr float LOG2E = 1.4426950408889634f;
constexpr unsigned WS_HF = 0, HF_BYTES = 32u * NJ * 4096u;
constexpr unsigned WS_MAIL = 24u << 20;
constexpr unsigned WS_Z = 40u << 20;
constexpr unsigned WS_FLAG = 41u << 20;
constexpr int NLOC = 17, HXOFF = NLOC * 8192, CTL = HXOFF + 18432, PFOFF = CTL + 256, LDS_BYTES = 163840;
constexpr int AVS = 48, AKX = HXOFF + 272 * AVS;
static_assert(AKX + 4096 + 256 <= CTL, "attention staging");
constexpr int OB0 = 6 * 18432, OB1 = 0, XOFF = 2 * 18432;

DI unsigned pk2(float lo, float hi) { f32x2 v = {lo, hi}; bf16x2 b = __builtin_convertvector(v, bf16x2); return __builtin_bit_cast(unsigned, b); }
DI bf16x4 trhalf(unsigned addr, int off) { return __builtin_amdgcn_ds_read_tr16_b64_v4bf16((LAS bf16x4*)(addr + off)); }
DI bf16x8 cat(bf16x4 a, bf16x4 b) { return __builtin_shufflevector(a, b, 0, 1, 2, 3, 4, 5, 6, 7); }
DI bf16x8 ld128(unsigned addr, int off) { return *(LAS bf16x8*)(addr + off); }
DI u32x4 mksrd(const void* p, unsigned bytes) { return (u32x4){(unsigned)(size_t)p, (unsigned)((size_t)p >> 32) & 0xffffu, bytes, 0x00020000u}; }
DI int lane_id() { int l = __builtin_amdgcn_mbcnt_hi(~0u, __builtin_amdgcn_mbcnt_lo(~0u, 0u)); asm volatile("" : "+v"(l)); return l; }
template <int L, int S> DI void bdma(unsigned lw, unsigned voff, u32x4 srd) {
  unsigned so;
  asm volatile("s_add_u32 m0, %1, %4\n\ts_mov_b32 %0, %5\n\tbuffer_load_dwordx4 %2, %3, %0 offen lds" : "=&s"(so) : "s"(lw), "v"(voff), "s"(srd), "i"(L), "i"(S) : "scc");
}
template <int L, int S> DI void bdma32(unsigned lw, unsigned voff, u32x4 srd) {
  unsigned so;
  asm volatile("s_add_u32 m0, %1, %4\n\ts_mov_b32 %0, %5\n\ts_mov_b32 exec_hi, 0\n\tbuffer_load_dwordx4 %2, %3, %0 offen lds\n\ts_mov_b32 exec_hi, -1" : "=&s"(so) : "s"(lw), "v"(voff), "s"(srd), "i"(L), "i"(S) : "scc");
}
template <int L, int S> DI void bdmar(unsigned lw, unsigned voff, u32x4 srd, unsigned run) {
  unsigned so;
  asm volatile("s_add_u32 m0, %1, %4\n\ts_add_u32 %0, %6, %5\n\tbuffer_load_dwordx4 %2, %3, %0 offen" NTW " lds" : "=&s"(so) : "s"(lw), "v"(voff), "s"(srd), "i"(L), "i"(S), "s"(run) : "scc");
}
template <int L, int S> DI void bdmar32(unsigned lw, unsigned voff, u32x4 srd, unsigned run) {
  unsigned so;
  asm volatile("s_add_u32 m0, %1, %4\n\ts_add_u32 %0, %6, %5\n\ts_mov_b32 exec_hi, 0\n\tbuffer_load_dwordx4 %2, %3, %0 offen" NTW " lds\n\ts_mov_b32 exec_hi, -1" : "=&s"(so) : "s"(lw), "v"(voff), "s"(srd), "i"(L), "i"(S), "s"(run) : "scc");
}
template <int L> DI void vdma(unsigned lw, const void* vaddr) {
  asm volatile("s_add_u32 m0, %0, %2\n\ts_nop 0\n\tglobal_load_lds_dwordx4 %1, off" NTW ::"s"(lw), "v"(vaddr), "i"(L) : "scc");
}
template <int N> DI void waitv() { asm volatile("s_waitcnt vmcnt(%0)" ::"n"(N > 63 ? 63 : N) : "memory"); }
DI void lds_arrive(unsigned addr, unsigned one) {
  asm volatile("s_mov_b64 exec, 1\n\tds_add_u32 %0, %1\n\ts_mov_b64 exec, -1" ::"v"(addr), "v"(one) : "memory");
}
template <int S, int O> DI void st16(unsigned voff, u32x4 srd, u32x4 v) {
  unsigned so;
  asm volatile("s_mov_b32 %0, %4\n\tbuffer_store_dwordx4 %1, %2, %3, %0 offen offset:%5" STP "\n\ts_nop 1" : "=&s"(so) : "v"(v), "v"(voff), "s"(srd), "i"(S), "i"(O) : "memory");
}
template <int O> DI void ld16s(bf16x8& d, unsigned voff, u32x4 srd, unsigned so) {
  asm volatile("buffer_load_dwordx4 %0, %1, %2, %3 offen offset:%4 sc1" : "=v"(d) : "v"(voff), "s"(srd), "s"(so), "i"(O));
}
template <int O> DI void ld8s(u32x2& d, unsigned voff, u32x4 srd, unsigned so) {
  asm volatile("buffer_load_dwordx2 %0, %1, %2, %3 offen offset:%4 sc1" : "=v"(d) : "v"(voff), "s"(srd), "s"(so), "i"(O));
}
template <int O> DI void st16s(unsigned voff, u32x4 srd, unsigned so, u32x4 v) {
  asm volatile("buffer_store_dwordx4 %0, %1, %2, %3 offen offset:%4 sc1\n\ts_nop 1" ::"v"(v), "v"(voff), "s"(srd), "s"(so), "i"(O) : "memory");
}
template <int O> DI void st8s(unsigned voff, u32x4 srd, unsigned so, u32x2 v) {
  asm volatile("buffer_store_dwordx2 %0, %1, %2, %3 offen offset:%4 sc1" ::"v"(v), "v"(voff), "s"(srd), "s"(so), "i"(O) : "memory");
}
template <int O> DI void st4s(unsigned voff, u32x4 srd, unsigned so, unsigned v) {
  asm volatile("buffer_store_dword %0, %1, %2, %3 offen offset:%4 sc1" ::"v"(v), "v"(voff), "s"(srd), "s"(so), "i"(O) : "memory");
}
template <char C, char B, bool Z> DI void mma(f32x4& acc, bf16x8 a, bf16x8 b) {
  if constexpr (C == 'v' && B == 'v' && !Z) asm volatile("v_mfma_f32_16x16x32_bf16 %0, %1, %2, %0" : "+v"(acc) : "v"(a), "v"(b));
  if constexpr (C == 'v' && B == 'v' && Z) asm volatile("v_mfma_f32_16x16x32_bf16 %0, %1, %2, 0" : "=v"(acc) : "v"(a), "v"(b));
  if constexpr (C == 'v' && B == 'a' && !Z) asm volatile("v_mfma_f32_16x16x32_bf16 %0, %1, %2, %0" : "+v"(acc) : "v"(a), "a"(b));
  if constexpr (C == 'v' && B == 'a' && Z) asm volatile("v_mfma_f32_16x16x32_bf16 %0, %1, %2, 0" : "=v"(acc) : "v"(a), "a"(b));
  if constexpr (C == 'a' && B == 'v' && !Z) asm volatile("v_mfma_f32_16x16x32_bf16 %0, %1, %2, %0" : "+a"(acc) : "v"(a), "v"(b));
  if constexpr (C == 'a' && B == 'a' && !Z) asm volatile("v_mfma_f32_16x16x32_bf16 %0, %1, %2, %0" : "+a"(acc) : "v"(a), "a"(b));
  if constexpr (C == 'a' && B == 'a' && Z) asm volatile("v_mfma_f32_16x16x32_bf16 %0, %1, %2, 0" : "=&a"(acc) : "v"(a), "a"(b));
}
DI void fix(bf16x8& a, bf16x8& b) {
  u32x4 x = __builtin_bit_cast(u32x4, a), y = __builtin_bit_cast(u32x4, b);
  asm volatile("" : "+v"(x), "+v"(y));
  unsigned x2 = x[2], x3 = x[3], y0 = y[0], y1 = y[1];
  asm volatile("v_swap_b32 %0, %2\n\tv_swap_b32 %1, %3\n\ts_nop 1" : "+v"(x2), "+v"(x3), "+v"(y0), "+v"(y1));
  x[2] = x2; x[3] = x3; y[0] = y0; y[1] = y1;
  a = __builtin_bit_cast(bf16x8, x); b = __builtin_bit_cast(bf16x8, y);
}
DI int kap(int rho) { return 8 * ((rho >> 2) & 3) + 4 * (rho >> 4) + (rho & 3); }
DI float red_max(float v) {
  auto a = __builtin_amdgcn_permlane16_swap(__float_as_uint(v), __float_as_uint(v), false, false);
  v = fmaxf(__uint_as_float(a[0]), __uint_as_float(a[1]));
  auto b = __builtin_amdgcn_permlane32_swap(__float_as_uint(v), __float_as_uint(v), false, false);
  return fmaxf(__uint_as_float(b[0]), __uint_as_float(b[1]));
}
DI float red_sum(float v) {
  auto a = __builtin_amdgcn_permlane16_swap(__float_as_uint(v), __float_as_uint(v), false, false);
  v = __uint_as_float(a[0]) + __uint_as_float(a[1]);
  auto b = __builtin_amdgcn_permlane32_swap(__float_as_uint(v), __float_as_uint(v), false, false);
  return __uint_as_float(b[0]) + __uint_as_float(b[1]);
}

#define PFST 13
constexpr int PD = 3;
constexpr int NS1 = 24, NG = 18, NST = NS1 + NG, NB1 = 54;
constexpr int LDH = 6;
enum { K_NORM = 0, K_SEL = 1, K_HALO = 2, K_G = 3 };
enum { V_NONE = 0, V_WB, V_XP, V_HX, V_ST, V_GW, V_GH, V_PL, V_PF, V_OW, V_OH };
enum { T_NONE = 0, T_EA, T_SIG, T_EB, T_EQ, T_ATT };
struct Unit { int stage, blk, ks, base, slot, kind, koff; };
constexpr int MAXU = 640;
struct Prog {
  int nu; Unit u[MAXU];
  int sfirst[NST + 1];
  int bstage[NB1 + NG], boff[NB1 + NG], biss[NB1 + NG];
  int vm[MAXU][4], va[MAXU][4];
  int ft[MAXU][4], fo[MAXU][4];
  int nwait[NST], nfirst;
  int pub[NST];
  int chk[MAXU];
  int chkw[MAXU];
  int tailops;
  bool ok;
};
constexpr int w0(const Prog& P, int n) { const int U = P.sfirst[n + 1] - P.sfirst[n]; return P.sfirst[n] + (U / 2 < U - PD ? U / 2 : U - PD); }
constexpr int ATT_PREP = 3, ATT_TILE = 12;
constexpr int ATT0 = 4 * ATT_PREP + 3, NATT = ATT0 + 4 * ATT_TILE + 1;
constexpr bool op_is_store(int task, int o) {
  if (task == T_EA) return o % 31 == 30;
  if (task == T_EB) return o % 7 == 6;
  if (task == T_EQ) return o % 31 == 30;
  if (task == T_ATT) { if (o < ATT0) return o == 4 * ATT_PREP; if (o == NATT - 1) return true; return (o - ATT0) % (2 * ATT_TILE) == 2 * ATT_TILE - 1; }
  return false;
}
constexpr Prog make_prog() {
  Prog P{}; P.ok = true;
  int nu = 0;
  auto add = [&](int st, int blk, int ks, int base, int slot, int kind, int koff) { P.u[nu] = Unit{st, blk, ks, base, slot, kind, koff}; ++nu; };
  for (int n = 0; n < 9; ++n) {
    P.sfirst[n] = nu;
    for (int u = 0; u < 16; ++u) { const int hp = u >> 3, kk = (u >> 2) & 1, pb = (u >> 1) & 1, isU = u & 1; add(n, 2 * n + isU, 2 * n + kk, 2 * hp + pb, 4 * hp + 2 * pb + isU, K_NORM, kk * 4096); }
    P.bstage[2 * n] = P.bstage[2 * n + 1] = n;
  }
  for (int n = 9; n < 15; ++n) {
    P.sfirst[n] = nu;
    const int e = (n - 9) % 3, fb = 18 + 3 * (n - 9), s0 = n < 12 ? 8 : 12;
    const bool skew = n == 9 || n == 14;
    for (int u = 0; u < 24; ++u) {
      const int ks = skew ? (u % 12) >> 1 : u >> 2, tile = skew ? 2 * (u / 12) + (u & 1) : u & 3;
      add(n, fb + (ks >> 1), 6 * e + ks, tile, s0 + tile, K_NORM, (ks & 1) * 4096);
    }
    for (int i = 0; i < 3; ++i) P.bstage[fb + i] = n;
  }
  for (int n = 15; n < 24; ++n) {
    P.sfirst[n] = nu;
    for (int kk = 0; kk < 2; ++kk) {
      const int ks = 2 * (n - 15) + kk, b = 36 + ks;
      add(n, b, ks, 4, 0, K_NORM, 0); add(n, b, ks, 5, 1, K_NORM, 0); add(n, b, ks, 6, 2, K_NORM, 0);
      add(n, b, ks, 8, 4, K_NORM, 0); add(n, b, ks, 9, 5, K_NORM, 0);
      add(n, b, ks, 7, 3, K_SEL, 0); add(n, b, ks, 10, 6, K_HALO, 0);
      P.bstage[b] = n;
    }
  }
  for (int i = 0; i < NG; ++i) {
    const int n = NS1 + i; P.sfirst[n] = nu;
    for (int j = 0; j < 9; ++j) add(n, NB1 + i, i, 11, j, K_G, 32 * j);
    P.bstage[NB1 + i] = n;
  }
  P.sfirst[NST] = nu; P.nu = nu;
  int freeat[NLOC] = {}; int cnt[NST + 1] = {};
  for (int l = 0; l < NLOC; ++l) freeat[l] = l < 9 ? -1 : 8;
  int prev = -1;
  for (int b = 0; b < NB1; ++b) {
    const int l = b <= 24 ? (b + 2) % 9 : (b - 16) % 17;
    int is = freeat[l] > prev ? freeat[l] : prev;
    if (b == 25) is = 8;
    while (is >= 0 && cnt[is] >= (is < 8 ? 2 : (is == 8 ? 4 : (is < 15 ? 6 : 3)))) ++is;
    if (is >= 0) ++cnt[is];
    P.boff[b] = l * 8192; P.biss[b] = is; prev = is; freeat[l] = P.bstage[b] + 1;
  }
  prev = 16;
  for (int i = 0; i < NG; ++i) {
    const int b = NB1 + i, off = ((i + 2) % 7) * 18432; int fr = prev;
    if (i < 7) { for (int l = off / 8192; l <= (off + 18431) / 8192; ++l) if (freeat[l] > fr) fr = freeat[l]; }
    else if (P.bstage[b - 7] + 1 > fr) fr = P.bstage[b - 7] + 1;
    P.boff[b] = off; P.biss[b] = fr; prev = fr;
    if (fr + 2 > P.bstage[b]) P.ok = false;
  }
  struct Op { int e, kind, arg; };
  Op sw[400] = {}, sx[200] = {}; int nw = 0, nx = 0;
  for (int b = 4; b < NB1; ++b) for (int h = 0; h < 2; ++h) sw[nw++] = Op{P.biss[b] < 0 ? 0 : w0(P, P.biss[b]), V_WB, 2 * b + h};
  for (int i = 0; i < NG; ++i) for (int p = 0; p < 5; ++p) sw[nw++] = Op{w0(P, P.biss[NB1 + i]), V_GW, 8 * i + p};
  for (int st = 0; st < 2; ++st) for (int p = 0; p < 10; ++p) sw[nw++] = Op{w0(P, NS1 + 12 + 2 * st), V_OW, 16 * st + p};
  for (int d = 2; d < 9; ++d) for (int i = 0; i < 8; ++i) sx[nx++] = Op{d == 2 ? 1 : P.sfirst[d - 3] + 9, V_XP, 8 * d + i};
  for (int i = 0; i < 5; ++i) sx[nx++] = Op{P.sfirst[7], V_HX, i};
  for (int i = PF0; i < 3; ++i) sx[nx++] = Op{P.sfirst[PFST] + 2 + 4 * i, V_PF, i};
  const int ck[5] = {0, NS1 - LDH, NS1 + 8 - LDH, NS1 + 13, NST};
  for (int n = NS1 - LDH - 2; n < NST; ++n) {
    for (int p = 1; p <= 4; ++p) {
      if (n == ck[p] - (p == 2 ? 3 : 2)) sx[nx++] = Op{P.sfirst[n], V_PL, p};
      if (n == ck[p]) P.chk[P.sfirst[n]] = p;
      if (p == 4) P.chk[nu] = 4;
    }
    for (int i = 0; i < NG; ++i) if (NS1 + i - (i >= 16 ? 3 : LDH) == n) for (int q = 0; q < 4; ++q) sx[nx++] = Op{P.sfirst[n], V_GH, 4 * i + q};
  }
  {
    int iw = 0, ix = 0; bool lastw = false;
    for (int U = 0; U < nu; ++U) {
      const int n = P.u[U].stage, lu = U - P.sfirst[n];
      if (P.u[U].kind == K_HALO || (n < 8 && lu >= 2 && lu < 6)) continue;
      const int per = (n < 2 || n == 8 || n == 9 || n == NS1 || n == NS1 + 1) ? 2 : 1;
      for (int k = 0; k < per; ++k) {
        const bool cw = iw < nw && sw[iw].e <= U, cx = ix < nx && sx[ix].e <= U;
        if (!cw && !cx) break;
        const bool usew = cw && (!cx || !lastw);
        const Op o = usew ? sw[iw++] : sx[ix++]; lastw = usew;
        const int t = per == 2 ? 1 + 2 * k : 3;
        P.vm[U][t] = o.kind; P.va[U][t] = o.arg;
      }
    }
    if (iw < nw || ix < nx) P.ok = false;
  }
  auto task = [&](int id, int nops, int U, int tmin, int o0 = 0) -> int {
    int t = 0;
    for (int o = o0; o < nops; ++o) {
      for (;;) {
        if (t < tmin) t = tmin;
        const bool halo = P.u[U].kind == K_HALO;
        const bool xrd = P.u[U].stage < 8 && U - P.sfirst[P.u[U].stage] >= 2 && U - P.sfirst[P.u[U].stage] < 6;
        if (!halo && !xrd && P.vm[U][t] == V_NONE && P.ft[U][t] == T_NONE) break;
        ++t; if (t > 3 || halo) { t = 0; ++U; }
      }
      P.ft[U][t] = id; P.fo[U][t] = o;
      if (op_is_store(id, o)) P.vm[U][t] = V_ST;
    }
    return U;
  };
  int e = 0;
  e = task(T_EA, 62, P.sfirst[8] + 9, 0);               if (e >= P.sfirst[9] + 12) P.ok = false;
  e = task(T_EA, 248, e + 1, 0, 62);                    if (e >= P.sfirst[12]) P.ok = false;
  const int endA = e;
  e = task(T_SIG, 96, P.sfirst[12] + 1, 1);             if (e >= P.sfirst[14] + 12) P.ok = false;
  const int es = e;
  e = task(T_EB, 28, P.sfirst[14] + 13, 0);             if (e >= P.sfirst[15]) P.ok = false;
  e = task(T_SIG, 192, es + 1, 0, 96);                  if (e >= P.sfirst[15]) P.ok = false;
  e = task(T_EB, 56, P.sfirst[15] + 1, 0, 28);
  const int endB = e;
  e = task(T_EQ, 62, P.sfirst[NS1] + 1, 0);
  const int endQ = e;
  e = task(T_ATT, ATT0 - 1, (e + 1 > w0(P, NS1) + 1 ? e + 1 : w0(P, NS1) + 1), 0);
  int sa = P.u[e].stage + (e == P.sfirst[P.u[e].stage] ? 0 : 1);
  e = task(T_ATT, NATT, w0(P, sa) + 1, 1, ATT0 - 1);
  if (P.u[e].stage >= NS1 + 11) P.ok = false;
  const int endO = e;
  int pos = 0; int posW[NB1 + NG] = {}, posX[9] = {}, posH[NG] = {}, posPL[5] = {}, poshx = -1, posU[MAXU + 1] = {}, posOW0 = -1;
  pos += 2; posW[0] = pos - 1; pos += 2; posW[1] = pos - 1; pos += 8; posX[0] = pos - 1;
  pos += 2; posW[2] = pos - 1; pos += 2; posW[3] = pos - 1; pos += 8; posX[1] = pos - 1;
  P.nfirst = pos - 1 - posX[0];
  for (int U = 0; U < nu; ++U) {
    posU[U] = pos;
    for (int t = 0; t < 4; ++t) {
      const int k = P.vm[U][t], a = P.va[U][t];
      if (k == V_NONE) continue;
      if (k == V_WB) posW[a >> 1] = pos;
      if (k == V_XP) posX[a >> 3] = pos;
      if (k == V_HX) poshx = pos;
      if (k == V_GW) posW[NB1 + (a >> 3)] = pos;
      if (k == V_GH) posH[a >> 2] = pos;
      if (k == V_PL) posPL[a] = pos;
      ++pos;
    }
  }
  posU[nu] = pos;
  int covered[NST] = {};
  for (int n = 0; n + 1 < NST; ++n) {
    int last = -1;
    for (int b = 0; b < NB1 + NG; ++b) if (P.bstage[b] == n + 1 && posW[b] > last) last = posW[b];
    if (n + 1 < 9 && posX[n + 1] > last) last = posX[n + 1];
    if (n + 1 == 15 && poshx > last) last = poshx;
    if (n + 1 >= NS1 && posH[n + 1 - NS1] > last) last = posH[n + 1 - NS1];
    int nw = posU[P.sfirst[n] + 1] - 1 - last;
    if (nw < 0) P.ok = false;
    P.nwait[n] = nw; covered[n] = last;
  }
  const int ends[5] = {0, endA, endB, endQ, endO};
  for (int lev = 1; lev <= 4; ++lev) {
    const int lastop = posU[ends[lev] + 1] - 1;
    int n = P.u[ends[lev]].stage + (lev == 1 ? 3 : (lev == 2 ? 4 : 5));
    for (int q = P.u[ends[lev]].stage + 1; q < n; ++q) if (covered[q] >= lastop) { n = q; break; }
    if (n + 1 >= NST) { P.ok = false; break; }
    if (covered[n] < lastop) { covered[n] = lastop; P.nwait[n] = posU[P.sfirst[n] + 1] - 1 - lastop; }
    if (P.pub[n] < lev) P.pub[n] = lev;
  }
  for (int U = 0; U <= nu; ++U) if (P.chk[U]) { P.chkw[U] = posU[U] - 1 - posPL[P.chk[U]]; if (P.chkw[U] < 0) P.ok = false; }
  (void)posOW0;
  return P;
}
constexpr Prog PG = make_prog();
static_assert(PG.ok, "schedule");
constexpr int rd_abs(int U) { return PG.boff[PG.u[U].blk] + PG.u[U].koff; }
constexpr int rd_win(int U) { return PG.u[U].kind == K_G ? (rd_abs(U) >= 73728 ? 73728 : 0) : (rd_abs(U) >> 16) << 16; }
constexpr int rd_prevwin(int U) { for (int q = U - 1; q >= 0; --q) if (PG.u[q].base == PG.u[U].base) return rd_win(q); return 0; }
constexpr int rd_h(int U) { return PG.u[U].kind == K_G ? 4608 : (PG.u[U].base < 4 ? 2048 : 4096); }
constexpr int g_j(int i) { return i < 16 ? 18 * ((i & 7) >> 2) + 4 * (i & 3) + 2 * (i >> 3) : 18 * (i - 16) + 16; }

__global__ __launch_bounds__(256, 1) void kf(const char* __restrict__ x, const char* __restrict__ wqkv, const char* __restrict__ wo, const char* __restrict__ wg,
                                              const char* __restrict__ wu, const char* __restrict__ wd, const char* __restrict__ gw, char* __restrict__ ws, char* __restrict__ out) {
  extern __shared__ __attribute__((aligned(16))) char smem[];
  const int tid = threadIdx.x; const int lane0 = tid & 63;
  const int wave = __builtin_amdgcn_readfirstlane(tid >> 6);
  const int bid = blockIdx.x, xcd = bid & 7, idx = bid >> 3;
  const int m = 4 * (xcd & 1) + (idx & 3), c = 8 * (xcd >> 1) + (idx >> 2), gam = c >> 2, ci = c & 3;
  const int s = xcd >> 1, mt = 8 * (xcd & 1) + (idx & 7), nt = idx >> 3;
  const int tg2 = wave & 1, kh = wave >> 1;
  const unsigned lbase = (unsigned)(size_t)smem;
  STAMP(0);
  const unsigned vx = (lane0 >> 3) * 1152 + (((lane0 & 7) ^ (lane0 >> 3)) << 4);
  const unsigned vw = kap(8 * wave + (lane0 >> 3)) * 9216 + (((((lane0 >> 1) & 3) ^ (lane0 >> 4)) << 1 | (lane0 & 1)) << 4);
  const unsigned wcol = 1152 * gam + 256 * ci;
  const u32x4 rx = mksrd(x + (size_t)(256 * m + 64 * wave) * 1152, 64 * 1152);
  const u32x4 rg = mksrd(wg + wcol, 576 * 9216 - wcol), ru = mksrd(wu + wcol, 576 * 9216 - wcol);
  const unsigned lw = lbase + 1024 * wave, lx = lbase + 9 * 8192 + 16384 * wave;
  const char* pq = nullptr; unsigned pqs = 0;
#define ROT1 ((2 * (m & 3) + (c & 7)) % 9)
  const unsigned rot = ROT1;
  unsigned runw = rot, runx = rot;
  auto adv = [&](unsigned& r) { r = r == 8 ? 0 : r + 1; asm volatile("" : "+s"(r)); };
  auto wblock = [&](auto A) {
    constexpr int b = decltype(A)::v >> 1, h2 = decltype(A)::v & 1, L = PG.boff[b] + h2 * 4096;
    if constexpr (b < 36) {
      const unsigned run = runw * (64 * 9216);
      if constexpr (b < 18) bdmar<L, h2 * (32 * 9216)>(lw, vw, (b & 1) ? ru : rg, run);
      else bdmar<L, 128 + h2 * (32 * 9216)>(lw, vw, b < 27 ? rg : ru, run);
      if constexpr (h2 == 1 && (b >= 18 || (b & 1))) adv(runw);
    } else {
      vdma<L>(lw, pq + (size_t)pqs * (runw * 16 + (b & 1) * 8 + h2));
      if constexpr (h2 == 1 && (b & 1)) adv(runw);
    }
  };
  auto xpiece = [&](auto A) { constexpr int d = decltype(A)::v >> 3, i = decltype(A)::v & 7; bdmar<(d & 1) * 8192 + 1024 * i, 8 * 1152 * i>(lx, vx, rx, runx * 128); if constexpr (i == 7) adv(runx); };
  sfor<4>(wblock);
  sfor<8>(xpiece);
  sfor<4>([&](auto A) { wblock(IC<4 + A.v>{}); });
  sfor<8>([&](auto A) { xpiece(IC<8 + A.v>{}); });
  if (tid < 64) *(LAS unsigned*)(lbase + CTL + 4 * lane0) = 0u;
  u64 tag = *(const __attribute__((address_space(4))) u64*)(size_t)(ws + WS_FLAG + 16384);
  tag = (tag ^ (tag >> 29)) * 0x9E3779B97F4A7C15ull + 0x632BE59BD9B4E019ull; tag ^= tag >> 32; tag *= 0xD6E8FEB86659FD93ull; tag &= ~7ull;
  const unsigned tlo = __builtin_amdgcn_readfirstlane((unsigned)tag), thi = __builtin_amdgcn_readfirstlane((unsigned)(tag >> 32));

  const unsigned axr = lx + (lane0 & 15) * 128 + (((lane0 >> 4) ^ (lane0 & 7)) << 4);
  unsigned acnt = lbase + CTL, one = 1u; PIN(acnt); PIN(one);
  const unsigned vh = lane0 * 16;
  const u32x4 rh = mksrd(ws + WS_HF, HF_BYTES);
  const u32x4 rhs = mksrd(ws + WS_HF + (size_t)((4 * m + wave) * NJ + 18 * gam + 4 * ci) * 4096, 4 * 4096);
  const u32x4 rf = mksrd(ws + WS_FLAG, 32768);
  unsigned bs[12], vg[5], ahx, av, avw, avr, avl, pmask, vo2, vpf[3], vpl8, vpl32;
  bool bok[4], bodd, bg0, brow0; const char* phx;
  {
    const int l2 = lane0, tau = l2 & 15, g = l2 >> 4, q4 = tau >> 2, p4 = l2 & 3;
    {
      const int r = 8 * (wave & 1) + (l2 >> 3); int tok = 256 * m - 16 + r; if (tok < 0) tok = 0;
      phx = (r == 0 ? gw : x + (size_t)tok * 1152) + (((l2 & 7) ^ (l2 >> 3)) << 4); PIN(phx);
    }
    const unsigned aw0 = lbase + (4 * g + q4) * 128 + (((2 * g + (q4 >> 1)) & 3) << 5) + 8 * p4;
#pragma unroll
    for (int i = 0; i < 4; ++i) bs[i] = aw0 ^ (i << 5);
    {
      const int row = 4 * wave + g, uq = (tau >> 1) ^ (row & 7), ch = (uq << 1) | (tau & 1), kr = kap(row);
      const bool gu = ch >= 6 && ch < 10;
      const int seg = ch < 6 ? ch >> 1 : (ch < 13 ? ch - 10 : 2);
      pqs = gu ? 4 * 9216 : 4 * 3456;
      pq = (gu ? (ch < 8 ? wg : wu) : wqkv) + (kr * (pqs >> 2) + (gu ? 1152 * gam + 1024 + 32 * ci + 16 * (ch & 1) : 1152 * seg + 36 * c + (ch < 6 ? 16 * (ch & 1) : 20)));
      PIN(pq); PIN(pqs);
    }
    const int rr = 4 * g + q4;
    const unsigned aq0 = lbase + rr * 256 + ((rr & 7) << 5) + 8 * p4;
    const unsigned aq3 = lbase + rr * 256 + (((p4 < 2 ? 5 : 6) ^ (rr & 7)) << 5) + ((p4 & 1) ? 24 : 8);
    bs[4] = aq0; bs[5] = aq0 ^ 32; bs[6] = aq0 ^ 64; bs[7] = aq3; bs[8] = aq0 ^ 96; bs[9] = aq0 ^ 128;
    bs[10] = wave == 0 ? bs[5] : (wave == 1 ? bs[6] : aq3);
    bs[11] = lbase + kh * 9216 + rr * 288 + 8 * p4;
#pragma unroll
    for (int i = 0; i < 12; ++i) PIN(bs[i]);
    ahx = lbase + HXOFF + tau * 128 + ((g ^ (l2 & 7)) << 4); PIN(ahx);
#pragma unroll
    for (int i = 0; i < 5; ++i) { const int q = 64 * i + (i == 4 ? (l2 & 31) : l2); vg[i] = (q / 18) * 1152 + (q % 18) * 16; PIN(vg[i]); }
    av = lbase + HXOFF + (16 + 64 * wave + tau) * AVS + 8 * g;
    avw = g == 2 ? lbase + HXOFF + (16 + 64 * wave + tau) * AVS + 32 : lbase + PFOFF + 1024 + 4 * l2;
    avl = lbase + HXOFF + (64 * wave + rr) * AVS + 32; avr = avl - 32 + 8 * p4;
    pmask = (tau & 1) ? 0xffff0000u : 0xffffu;
    vo2 = (g >> 1) * 128 + tau * 8 + (g & 1) * 4;
    const int qq = idx * 4 + wave;
    vpf[0] = (qq * 81 + l2) * 128; vpf[1] = vpf[0] + 17 * 128; vpf[2] = (qq * 41 + (l2 < 41 ? l2 : 40)) * 128;
    vpl8 = ((mt >> 1) * 32 + 8 * s + (l2 & 7)) * 8; vpl32 = ((mt >> 1) * 32 + (l2 & 31)) * 8;
    PIN(av); PIN(avw); PIN(avl); PIN(avr); PIN(pmask); PIN(vo2); PIN(vpf[0]); PIN(vpf[2]); PIN(vpl8); PIN(vpl32);
    bodd = tau & 1; bg0 = g == 0; brow0 = tau == 0;
#pragma unroll
    for (int i = 0; i < 4; ++i) bok[i] = (unsigned)(tau + 16 - (16 * (i >> 1) + 4 * g + 2 * (i & 1) + (tau & 1))) <= 14u;
  }
  u64 mk[7] = {__ballot(bok[0]), __ballot(bok[1]), __ballot(bok[2]), __ballot(bok[3]), __ballot(bodd), __ballot(bg0), __ballot(brow0)};
  auto sel = [&](u64 mask, unsigned a1, unsigned a0_) -> unsigned { unsigned r; asm volatile("v_cndmask_b32 %0, %1, %2, %3" : "=v"(r) : "v"(a0_), "v"(a1), "s"(mask)); return r; };
  const unsigned od = (unsigned)(36 * s + kh) * (32 * 1152) + 16 * tg2 * 1152 + 288 * nt;
  u32x4 rd = mksrd(wd + od, 4608 * 1152 - od);
  const u32x4 rgh = mksrd(ws + WS_HF + (size_t)((2 * mt + tg2) * NJ + 36 * s + kh) * 4096, 36 * 4096);
  const unsigned oo = (unsigned)(36 * wave) * 1152 + 288 * nt;
  u32x4 ro = mksrd(wo + oo, 576 * 1152 - oo);
  asm volatile("" : "+s"(rd), "+s"(ro), "+s"(wo), "+s"(wd), "+s"(out));
  bf16x8 xf[18][4], hr[NG][4], of[5][2], xh, xhn;
  f32x4 acc[16][4], acch, ga[9][4];
  u32x4 hw = {0u, 0u, 0u, 0u};
  u32x2 pl[5]; f32x2 zv;
  const f32x4 zero = {0.f, 0.f, 0.f, 0.f};

#define ROT2 (2 * nt + mt)
  const unsigned rot2 = ROT2;
  unsigned gwo = 0, gho = 0;
  auto gj = [&](int i) -> unsigned {
    if (i < 16) { const unsigned p = (i + rot2) & 7; return 18 * (p >> 2) + 4 * (p & 3) + 2 * (i >> 3); }
    return 18 * ((i + rot2) & 1) + 16;
  };
  auto oblock = [&](auto ST, auto PP, auto LL, unsigned ldst) {
    constexpr int st = decltype(ST)::v, h2 = decltype(PP)::v / 5, i = decltype(PP)::v % 5, L = decltype(LL)::v + h2 * 4608 + 1024 * i, S = (144 * st + 18 * h2) * 1152;
    if constexpr (i < 4) bdma<L, S>(ldst, vg[i], ro); else bdma32<L, S>(ldst, vg[4], ro);
  };
  auto whalf = [&](auto UU, int h) -> bf16x4 {
    constexpr int U = decltype(UU)::v, bi = PG.u[U].base, win = rd_win(U), pw = rd_prevwin(U);
    if (h == 0 && win != pw) { bs[bi] += (unsigned)(win - pw); PIN(bs[bi]); }
    return trhalf(bs[bi], rd_abs(U) - win + h * rd_h(U));
  };
  const float nl2e = -LOG2E;
  auto eop = [&](f32x4& gq, f32x4& uq, int op, int half) {
    if (op < 2 || (op >= 6 && op < 8) || (op >= 12 && op < 14)) {
      const int i = 2 * (op & 1); float a = gq[i], b = gq[i + 1];
      if (op < 2) asm volatile("v_mul_f32 %0, %2, %0\n\tv_mul_f32 %1, %2, %1" : "+v"(a), "+v"(b) : "s"(nl2e));
      else if (op < 8) asm volatile("v_add_f32 %0, 1.0, %0\n\tv_add_f32 %1, 1.0, %1" : "+v"(a), "+v"(b));
      else asm volatile("v_mul_f32 %0, %2, %0\n\tv_mul_f32 %1, %3, %1" : "+v"(a), "+v"(b) : "v"(uq[i]), "v"(uq[i + 1]));
      gq[i] = a; gq[i + 1] = b;
    } else if (op < 12) {
      const int i = op < 6 ? op - 2 : op - 8; float a = gq[i];
      if (op < 6) asm volatile("v_exp_f32 %0, %0" : "+v"(a)); else asm volatile("v_rcp_f32 %0, %0" : "+v"(a));
      gq[i] = a;
    } else {
      unsigned a, b;
      asm volatile("v_cvt_pk_bf16_f32 %0, %2, %3\n\tv_cvt_pk_bf16_f32 %1, %4, %5" : "=&v"(a), "=v"(b) : "v"(gq[0]), "v"(gq[1]), "v"(gq[2]), "v"(gq[3]));
      hw[2 * half] = a; hw[2 * half + 1] = b;
    }
  };
  u32x4 qf[4], kf[4], kp; bf16x8 vf0, vf1; f32x4 s0, s1, o0, o1; float cv[4], mx, sm; u32x4 pw; unsigned ol = 0;

  auto check = [&](auto UU) {
    constexpr int U = decltype(UU)::v, p = PG.chk[U];
    asm volatile("s_waitcnt vmcnt(%1)" : "+v"(pl[p]) : "n"(PG.chkw[U] > 63 ? 63 : PG.chkw[U]) : "memory");
    if (__builtin_expect(!__all(pl[p][1] == thi && pl[p][0] - tlo - p <= 7u - p), 0)) {
      const u64* fp = (const u64*)(ws + WS_FLAG + (p == 4 ? vpl32 : vpl8));
      for (unsigned spin = 0; spin < (1u << 14); ++spin) {
        const u64 v = __hip_atomic_load(fp, __ATOMIC_RELAXED, __HIP_MEMORY_SCOPE_AGENT);
        if (__all((unsigned)(v >> 32) == thi && (unsigned)v - tlo - p <= 7u - p)) break;
        __builtin_amdgcn_s_sleep(2);
      }
    }
    asm volatile("" ::: "memory");
  };
  waitv<PG.nfirst>();
  asm volatile("s_waitcnt lgkmcnt(0)" ::: "memory");
  BAR();
  STAMP(1);
#pragma unroll
  for (int i = 0; i < 8; ++i) xf[i >> 2][i & 3] = ld128(axr ^ ((i >> 2) << 6), (i & 3) * 2048);
  bf16x4 w[PG.nu + PD][2];
  sfor<PD>([&](auto J) { w[J.v][0] = whalf(J, 0); w[J.v][1] = whalf(J, 1); });
  unsigned pv = 0;

  sfor<PG.nu>([&](auto UU) {
    constexpr int U = decltype(UU)::v; constexpr Unit un = PG.u[U];
    constexpr int n = un.stage, lu = U - PG.sfirst[n], W0 = w0(PG, n);
    if constexpr (lu == 0 && n == 9) STAMP(2);
    if constexpr (lu == 0 && n == 12) STAMP(11);
    if constexpr (lu == 0 && n == NS1 + 9) STAMP(12);
    if constexpr (lu == 0 && n == 15) STAMP(3);
    if constexpr (lu == 0 && n == NS1) STAMP(4);
    if constexpr (lu == 1 && n + 1 < NST) { waitv<PG.nwait[n]>(); lds_arrive(acnt, one); }
    if constexpr (U == W0 - 1 && n + 1 < NST) pv = *(volatile LAS unsigned*)(acnt);
    if constexpr (U == W0 && n + 1 < NST) {
      if (__builtin_expect((unsigned)__builtin_amdgcn_readfirstlane(pv) < 4u * (n + 1), 0)) {
        do { pv = *(volatile LAS unsigned*)(acnt); } while ((unsigned)__builtin_amdgcn_readfirstlane(pv) < 4u * (n + 1));
      }
      asm volatile("" ::: "memory");
      if constexpr (PG.pub[n] != 0) {
        if (tid == 0) __hip_atomic_store((u64*)(ws + WS_FLAG) + (m * 32 + c), ((u64)thi << 32) | (tlo + PG.pub[n]), __ATOMIC_RELAXED, __HIP_MEMORY_SCOPE_AGENT);
      }
    }
    if constexpr (PG.chk[U] != 0) check(UU);
    if constexpr (un.kind == K_G && lu == 0 && un.ks < 16) asm volatile("" : "+a"(hr[un.ks][0]), "+a"(hr[un.ks][1]), "+a"(hr[un.ks][2]), "+a"(hr[un.ks][3]));
    if constexpr (un.kind == K_G && lu == 3 && un.ks >= 15 && un.ks < 17) { fix(hr[un.ks + 1][0], hr[un.ks + 1][1]); fix(hr[un.ks + 1][2], hr[un.ks + 1][3]); }
    SB();
    bf16x8 a = cat(w[U][0], w[U][1]);
    if constexpr (un.kind == K_SEL) {
      u32x4 avv = __builtin_bit_cast(u32x4, a), hv = __builtin_bit_cast(u32x4, xh);
#pragma unroll
      for (int e = 0; e < 4; ++e) avv[e] = sel(mk[6], hv[e], avv[e]);
      a = __builtin_bit_cast(bf16x8, avv);
      asm volatile("s_nop 1" : "+v"(a));
    }
    constexpr int UN = U + PD;
    if constexpr (un.kind == K_HALO) {
      mma<'v', 'v', un.ks == 0>(acch, a, xh);
      SB();
      w[UN][0] = whalf(IC<UN>{}, 0); w[UN][1] = whalf(IC<UN>{}, 1);
      xh = xhn;
      SB();
    } else {
      sfor<4>([&](auto T) {
        constexpr int t = decltype(T)::v;
        if constexpr (un.kind == K_G) mma<(t < 2 ? 'v' : 'a'), (un.ks < 16 ? 'a' : 'v'), un.ks == 0>(ga[un.slot][t], a, hr[un.ks][t]);
        else mma<'v', (un.ks < 16 ? 'a' : 'v'), un.ks == 0>(acc[un.slot][t], a, xf[un.ks][t]);
        SB();
        if constexpr (t < 2 && UN < PG.nu) w[UN][t] = whalf(IC<(UN < PG.nu ? UN : 0)>{}, t);
        if constexpr (t >= 2 && n < 8 && lu >= 2 && lu < 6) xf[2 * n + t][lu - 2] = ld128(axr ^ ((t - 2) << 6), ((n + 1) & 1) * 8192 + (lu - 2) * 2048);
        if constexpr (t == 2 && n == 14 && lu == 20) xh = ld128(ahx, 0);
        if constexpr (t == 2 && n >= 15 && n < NS1 && un.base == 8 && un.ks < 17) xhn = ld128(ahx ^ (((un.ks + 1) & 1) << 6), ((un.ks + 1) >> 1) * 2048);
                constexpr int vk = PG.vm[U][t], va = PG.va[U][t];
        if constexpr (vk == V_WB) wblock(IC<va>{});
        if constexpr (vk == V_XP) xpiece(IC<va>{});
        if constexpr (vk == V_HX) {
          int pc = wave + 4 * va; if (pc >= 18) pc -= 4;
          unsigned dd = (pc >> 1) + rot; if (dd >= 9) dd -= 9;
          vdma<HXOFF>(lbase + pc * 1024, phx + 128 * dd);
        }
        if constexpr (vk == V_PF) {
          const u32x4 rp = va < 2 ? mksrd(wd + (size_t)s * (1152 * 1152) + va * (17 * 128), 1152 * 1152 - va * (17 * 128)) : mksrd(wo, 576 * 1152);
          asm volatile("s_mov_b32 m0, %0\n\ts_nop 0\n\tbuffer_load_dword %1, %2, 0 offen lds" ::"s"(lbase + PFOFF + 256 * wave), "v"(vpf[va < 2 ? 0 : 2]), "s"(rp));
        }
        if constexpr (vk == V_GW) {
          constexpr int i = va >> 3, p = va & 7, L = PG.boff[NB1 + i] + 1024 * p;
          const unsigned lg = lbase + kh * 9216 + tg2 * 4608;
          if constexpr (p == 0) { gwo = gj(i) * (32 * 1152); asm volatile("" : "+s"(gwo)); }
          if constexpr (p < 4) bdmar<L, 0>(lg, vg[p], rd, gwo); else bdmar32<L, 0>(lg, vg[4], rd, gwo);
        }
        if constexpr (vk == V_GH) {
          constexpr int i = va >> 2, q = va & 3;
          if constexpr ((q & 1) == 0) { gho = gj(i) * 4096 + ((q >> 1) ^ kh) * 2048; asm volatile("" : "+s"(gho)); }
                    if constexpr (i < 16) asm volatile("buffer_load_dwordx4 %0, %1, %2, %3 offen offset:%4 sc1" : "=a"(hr[i][q]) : "v"(vh), "s"(rgh), "s"(gho), "i"((q & 1) * 1024));
          else asm volatile("buffer_load_dwordx4 %0, %1, %2, %3 offen offset:%4 sc1" : "=v"(hr[i][q]) : "v"(vh), "s"(rgh), "s"(gho), "i"((q & 1) * 1024));
        }
        if constexpr (vk == V_PL) asm volatile("buffer_load_dwordx2 %0, %1, %2, 0 offen sc1" : "=v"(pl[va]) : "v"(va == 4 ? vpl32 : vpl8), "s"(rf));
        if constexpr (vk == V_OW) oblock(IC<(va >> 4)>{}, IC<(va & 15)>{}, IC<((va >> 4) == 0 ? OB0 : OB1)>{}, lbase + wave * 9216);
                constexpr int ft = PG.ft[U][t], fo = PG.fo[U][t];
        if constexpr (ft == T_EA) {
          constexpr int grp = fo / 31, hp = grp >> 2, t2 = grp & 3, o = fo % 31;
          if constexpr (o < 30) eop(acc[4 * hp + 2 * (o / 15)][t2], acc[4 * hp + 2 * (o / 15) + 1][t2], o % 15, o / 15);
          else st16<hp * 4096, t2 * 1024>(vh, rhs, hw);
        }
        if constexpr (ft == T_SIG) { constexpr int q = fo / 12; eop(acc[8 + 2 * (q >> 3) + (q & 1)][(q >> 1) & 3], acch, fo % 12, 0); }
        if constexpr (ft == T_EB) {
          constexpr int grp = fo / 7, hp = grp >> 2, t2 = grp & 3, o = fo % 7;
          if constexpr (o < 6) eop(acc[8 + 2 * hp + o / 3][t2], acc[12 + 2 * hp + o / 3][t2], 12 + o % 3, o / 3);
          else st16<(2 + hp) * 4096, t2 * 1024>(vh, rhs, hw);
        }
        if constexpr (ft == T_EQ) {
          constexpr int tp = fo / 31, o = fo % 31;
          if constexpr (o < 30) eop(acc[4][2 * tp + o / 15], acc[5][2 * tp + o / 15], o % 15, o / 15);
          else st16s<tp * 2048>(vh, rh, (unsigned)(((4 * m + wave) * NJ + 18 * gam + 16 + (ci >> 1)) * 4096 + 1024 * (ci & 1)), hw);
        }
        if constexpr (ft == T_ATT) {
          if constexpr (fo < 4 * ATT_PREP) {
            constexpr int t2 = fo / ATT_PREP, o = fo % ATT_PREP;
            if constexpr (o == 0) { qf[t2][0] = pk2(acc[0][t2][0], acc[0][t2][1]); qf[t2][1] = pk2(acc[0][t2][2], acc[0][t2][3]); kf[t2][0] = pk2(acc[1][t2][0], acc[1][t2][1]); kf[t2][1] = pk2(acc[1][t2][2], acc[1][t2][3]); }
            if constexpr (o == 1) {
              PIN(acc[3][t2]);
              const unsigned L = pk2(acc[3][t2][2], acc[3][t2][3]);
              *(LAS unsigned*)(avw + t2 * 16 * AVS) = L;
              auto r = __builtin_amdgcn_permlane16_swap(L, L, false, false);
              qf[t2][2] = sel(mk[5], r[0], 0u); qf[t2][3] = 0u; kf[t2][2] = r[1]; kf[t2][3] = 0u; PIN(qf[t2]); PIN(kf[t2]);
            }
            if constexpr (o == 2) { *(LAS u32x2*)(av + t2 * 16 * AVS) = (u32x2){pk2(acc[2][t2][0], acc[2][t2][1]), pk2(acc[2][t2][2], acc[2][t2][3])}; }
          } else if constexpr (fo == 4 * ATT_PREP) {
            st16s<0>(vh, mksrd(ws + WS_Z + (4 * m + wave) * 256, c == 0 ? 256u : 0u), 0u,
                     (u32x4){__float_as_uint(acc[3][0][0]), __float_as_uint(acc[3][1][0]), __float_as_uint(acc[3][2][0]), __float_as_uint(acc[3][3][0])});
          } else if constexpr (fo == 4 * ATT_PREP + 1) {
            const int l2 = lane_id();
            if (wave < 3) *(LAS u32x4*)(lbase + AKX + (wave + 1) * 1024 + l2 * 16) = kf[3];
            const u32x2 hp = {pk2(acch[0], acch[1]), pk2(acch[2], acch[3])};
            auto r = __builtin_amdgcn_permlane16_swap(hp[1], hp[1], false, false);
            if (wave == 0) *(LAS u32x2*)(lbase + AKX + l2 * 16) = hp;
            if (wave == 1) *(LAS u32x2*)(av - 80 * AVS) = hp;
            if (wave == 2) { *(LAS u32x2*)(lbase + AKX + l2 * 16 + 8) = (u32x2){r[1], 0u}; if ((l2 >> 4) == 2) *(LAS unsigned*)(avw - 144 * AVS) = hp[1]; }
            asm volatile("s_waitcnt lgkmcnt(0)" ::: "memory");
          } else if constexpr (fo == 4 * ATT_PREP + 2) {
            kp = *(LAS u32x4*)(lbase + AKX + wave * 1024 + vh);
          } else if constexpr (fo == NATT - 1) {
            st4s<0>(vo2, rh, (unsigned)(((4 * m + wave) * NJ + 160) * 4096 + 256 * c), ol);
          } else {
            constexpr int q = fo - ATT0, t2 = q / ATT_TILE, o = q % ATT_TILE;
            static_assert(true, "");
            if constexpr (o == 0) {
              mma<'v', 'v', true>(s0, __builtin_bit_cast(bf16x8, t2 == 0 ? kp : kf[t2 == 0 ? 0 : t2 - 1]), __builtin_bit_cast(bf16x8, qf[t2]));
              mma<'v', 'v', true>(s1, __builtin_bit_cast(bf16x8, kf[t2]), __builtin_bit_cast(bf16x8, qf[t2]));
            }
            if constexpr (o == 1) { vf0 = cat(trhalf(avr, t2 * 16 * AVS), trhalf(avr, (t2 + 1) * 16 * AVS)); vf1 = cat(trhalf(avl, t2 * 16 * AVS), trhalf(avl, (t2 + 1) * 16 * AVS)); }
            if constexpr (o == 2) {
              asm volatile("s_nop 3" : "+v"(s0), "+v"(s1));
#pragma unroll
              for (int i = 0; i < 4; ++i) { const f32x4& sq = i < 2 ? s0 : s1; cv[i] = __uint_as_float(sel(mk[4], __float_as_uint(sq[2 * (i & 1) + 1]), __float_as_uint(sq[2 * (i & 1)]))); }
            }
            if constexpr (o == 3) {
              const bool first = t2 == 0 && m == 0 && wave == 0;
#pragma unroll
              for (int i = 0; i < 4; ++i) cv[i] = __uint_as_float(sel((first && i < 2) ? 0ull : mk[i], __float_as_uint(cv[i]), 0xff61b1e6u));
            }
            if constexpr (o == 4) { mx = red_max(fmaxf(fmaxf(cv[0], cv[1]), fmaxf(cv[2], cv[3]))); PIN(mx); }
            if constexpr (o == 5) {
              constexpr float SC = 0.23570226039551584f * LOG2E;
#pragma unroll
              for (int i = 0; i < 4; ++i) { cv[i] = (cv[i] - mx) * SC; PIN(cv[i]); }
            }
            if constexpr (o == 6) {
#pragma unroll
              for (int i = 0; i < 4; ++i) { cv[i] = __builtin_amdgcn_exp2f(cv[i]); PIN(cv[i]); }
            }
            if constexpr (o == 7) {
#pragma unroll
              for (int i = 0; i < 4; ++i) pw[i] = pk2(cv[i], cv[i]) & pmask;
              PIN(pw);
            }
            if constexpr (o == 8) {
              asm volatile("s_nop 0" : "+v"(pw)); mma<'v', 'v', true>(o0, vf0, __builtin_bit_cast(bf16x8, pw)); mma<'v', 'v', true>(o1, vf1, __builtin_bit_cast(bf16x8, pw));
            }
            if constexpr (o == 9) { sm = __builtin_amdgcn_rcpf(red_sum((cv[0] + cv[1]) + (cv[2] + cv[3]))); PIN(sm); }
            if constexpr (o == 10) {
              hw[2 * (t2 & 1)] = pk2(o0[0] * sm, o0[1] * sm); hw[2 * (t2 & 1) + 1] = pk2(o0[2] * sm, o0[3] * sm); PIN(hw);
              PIN(o1);
              const unsigned lv = pk2(o1[0] * sm, o1[1] * sm);
              ol = t2 == 0 ? lv : sel(0xffffull << (16 * t2), lv, ol); PIN(ol);
            }
            if constexpr (o == 11 && (t2 & 1)) st16s<(t2 >> 1) * 2048>(vh, rh, (unsigned)(((4 * m + wave) * NJ + 144 + (c >> 1)) * 4096 + 1024 * (c & 1)), hw);
          }
        }
        SB();
      });
    }
  });
  const int lane = lane_id();
  check(IC<PG.nu>{});
  u32x2 lf[4];
  {
    const unsigned so = (unsigned)(((2 * mt + (s >> 1)) * NJ + 144 + wave) * 4 + 2 * (s & 1)) * 1024;
    sfor<8>([&](auto Q) { constexpr int st = Q.v >> 1, q = Q.v & 1; ld16s<q * 1024>(of[st][q], vh, rh, so + st * 16384); });
    const unsigned sl = (unsigned)(((2 * mt + (s >> 1)) * NJ + 160 + (wave & 1)) * 4096 + 128 * (s & 1));
    const unsigned vl = (lane >> 4) * 1024 + (lane & 15) * 8;
    sfor<4>([&](auto Q) { ld8s<Q.v * 256>(lf[Q.v], vl, rh, sl); });
    asm volatile("buffer_load_dwordx2 %0, %1, %2, 0 offen sc1" : "=v"(zv) : "v"((lane & 15) * 16), "s"(mksrd(ws + WS_Z + (2 * mt + (s >> 1)) * 256 + 8 * (s & 1), 256)));
  }
  asm volatile("s_nop 7\n\ts_waitcnt lgkmcnt(0)" ::: "memory");
  BAR();
  SB();
  STAMP(5);
  const int grp = mt * 4 + nt, quarter = 2 * tg2 + kh;
  const unsigned xw = lbase + XOFF + wave * 18432, xr = lbase + XOFF + (wave ^ 2) * 18432;
#pragma unroll
  for (int j = 0; j < 9; ++j) { *(LAS f32x4*)(xw + lane * 16 + j * 2048) = ga[j][2]; *(LAS f32x4*)(xw + lane * 16 + j * 2048 + 1024) = ga[j][3]; }
  asm volatile("s_waitcnt lgkmcnt(0)" ::: "memory");
  BAR();
#pragma unroll
  for (int j = 0; j < 9; ++j) { ga[j][0] += *(LAS f32x4*)(xr + lane * 16 + j * 2048); ga[j][1] += *(LAS f32x4*)(xr + lane * 16 + j * 2048 + 1024); }
  const u32x4 rm = mksrd(ws + WS_MAIL + (size_t)grp * (16 * 9216), 16 * 9216);
  {
    const unsigned vo = lane * 16 + (quarter * 4 + s) * 9216;
    sfor<9>([&](auto J) { constexpr int j = J.v;
      st16<(j >> 2) * 4096, (j & 3) * 1024>(vo, rm, (u32x4){pk2(ga[j][0][0], ga[j][0][1]), pk2(ga[j][0][2], ga[j][0][3]), pk2(ga[j][1][0], ga[j][1][1]), pk2(ga[j][1][2], ga[j][1][3])}); });
  }
  asm volatile("s_waitcnt lgkmcnt(0)" ::: "memory");
  SB();
  STAMP(6);
  sfor<20>([&](auto Q) { oblock(IC<2 + Q.v / 10>{}, IC<Q.v % 10>{}, IC<(Q.v / 10) * 9216>{}, xr); });
  f32x4 oa[9][2];
  const unsigned trw = (4 * (lane >> 4) + ((lane & 15) >> 2)) * 288 + 8 * (lane & 3);
  auto ostep = [&](auto ST, unsigned base) {
    constexpr int st = decltype(ST)::v;
    bf16x8 wf[9];
#pragma unroll
    for (int j = 0; j < 9; ++j) wf[j] = cat(trhalf(base, 32 * j), trhalf(base, 32 * j + 4608));
#pragma unroll
    for (int j = 0; j < 9; ++j) { mma<'v', 'v', st == 0>(oa[j][0], wf[j], of[st][0]); mma<'v', 'v', st == 0>(oa[j][1], wf[j], of[st][1]); }
  };
  asm volatile("s_waitcnt vmcnt(29)" : "+v"(of[0][0]), "+v"(of[0][1]), "+v"(of[1][0]), "+v"(of[1][1]), "+v"(of[2][0]), "+v"(of[2][1]), "+v"(of[3][0]), "+v"(of[3][1]),
               "+v"(lf[0]), "+v"(lf[1]), "+v"(lf[2]), "+v"(lf[3]), "+v"(zv) :: "memory");
#pragma unroll
  for (int st = 0; st < 4; ++st) fix(of[st][0], of[st][1]);
#pragma unroll
  for (int t = 0; t < 2; ++t) { u32x4 v = {lf[0][t], lf[1][t], lf[2][t], lf[3][t]}; asm volatile("s_nop 1" : "+v"(v)); of[4][t] = __builtin_bit_cast(bf16x8, v); }
  ostep(IC<0>{}, lbase + OB0 + wave * 9216 + trw);
  ostep(IC<1>{}, lbase + OB1 + wave * 9216 + trw);
  asm volatile("s_waitcnt vmcnt(20) lgkmcnt(0)" ::: "memory");
  if (lane == 0)
    __hip_atomic_store((u64*)(ws + WS_FLAG + 4096 + grp * 128) + (quarter * 4 + s), ((u64)thi << 32) | (tlo + 1u), __ATOMIC_RELAXED, __HIP_MEMORY_SCOPE_AGENT);
  STAMP(7);
  if (wave < 2) {
    const unsigned o = (unsigned)(18 * 16 * wave) * 1152 + 288 * nt;
    const u32x4 rl = mksrd(wo + o, 576 * 1152 - o);
    sfor<9>([&](auto Q) { constexpr int p = Q.v;
      const int q = 64 * p + lane, rho = q / 18, e = 4 * (rho >> 4) + (rho & 3), gg = (rho >> 2) & 3;
      bdma<OB0 + 1024 * p, 0>(lbase + wave * 9216, (unsigned)((18 * (4 * gg + (e >> 1)) + 16 + (e & 1)) * 1152 + (q % 18) * 16), rl); });
    asm volatile("s_waitcnt vmcnt(9)" ::: "memory");
  } else asm volatile("s_waitcnt vmcnt(0)" ::: "memory");
  ostep(IC<2>{}, xr + trw);
  ostep(IC<3>{}, xr + 9216 + trw);
  if (wave < 2) { asm volatile("s_waitcnt vmcnt(0)" ::: "memory"); ostep(IC<4>{}, lbase + OB0 + wave * 9216 + trw); }
  asm volatile("s_nop 7\n\ts_waitcnt lgkmcnt(0)" ::: "memory");
  SB();
  float gt[2], om[2];
#pragma unroll
  for (int t = 0; t < 2; ++t) { gt[t] = __builtin_amdgcn_rcpf(1.f + __builtin_amdgcn_exp2f(zv[t] * -LOG2E)); om[t] = __builtin_amdgcn_rcpf(1.f + __builtin_amdgcn_exp2f(zv[t] * LOG2E)); }
  if (quarter == s) {
#pragma unroll
    for (int j = 0; j < 9; ++j)
#pragma unroll
      for (int t = 0; t < 2; ++t) *(LAS f32x4*)(xr + lane * 16 + j * 2048 + t * 1024) = oa[j][t] * gt[t] + ga[j][t] * om[t];
  } else {
#pragma unroll
    for (int j = 0; j < 9; ++j)
#pragma unroll
      for (int t = 0; t < 2; ++t) *(LAS f32x4*)(xr + lane * 16 + j * 2048 + t * 1024) = oa[j][t] * gt[t];
  }
  asm volatile("s_waitcnt lgkmcnt(0)" ::: "memory");
  BAR();
  STAMP(8);
  f32x4 fb[3][2];
#pragma unroll
  for (int i = 0; i < 3; ++i) {
    const int j = wave + 4 * i < 9 ? wave + 4 * i : 8;
#pragma unroll
    for (int t = 0; t < 2; ++t) {
      const unsigned aa = lbase + XOFF + lane * 16 + j * 2048 + t * 1024;
      fb[i][t] = (*(LAS f32x4*)(aa) + *(LAS f32x4*)(aa + 18432)) + (*(LAS f32x4*)(aa + 36864) + *(LAS f32x4*)(aa + 55296));
    }
  }
  {
    const u64* fp = (const u64*)(ws + WS_FLAG + 4096 + grp * 128) + s * 4 + (lane & 3);
    for (unsigned spin = 0; spin < (1u << 14); ++spin) {
      const u64 v = __hip_atomic_load(fp, __ATOMIC_RELAXED, __HIP_MEMORY_SCOPE_AGENT);
      if (__all((lane & 3) == s || v == (((u64)thi << 32) | (tlo + 1u)))) break;
      __builtin_amdgcn_s_sleep(1);
    }
    asm volatile("" ::: "memory");
  }
  STAMP(9);
  {
    const __amdgpu_buffer_rsrc_t mail = __builtin_amdgcn_make_buffer_rsrc(ws + WS_MAIL + (size_t)grp * (16 * 9216), 0, 16 * 9216, 0x00020000);
    u32x4 mv[3][3];
#pragma unroll
    for (int i = 0; i < 3; ++i) {
      const int j = wave + 4 * i < 9 ? wave + 4 * i : 8;
#pragma unroll
      for (int o = 0; o < 3; ++o) mv[i][o] = __builtin_amdgcn_raw_buffer_load_b128(mail, lane * 16, (s * 4 + o + (o >= s)) * 9216 + j * 1024, 16);
    }
#pragma unroll
    for (int i = 0; i < 3; ++i) {
      const int j = wave + 4 * i;
      if (j < 9) {
#pragma unroll
        for (int t = 0; t < 2; ++t) {
          float r[4];
#pragma unroll
          for (int e = 0; e < 4; ++e) {
            float sum = 0.f;
#pragma unroll
            for (int o = 0; o < 3; ++o) { const unsigned d = mv[i][o][2 * t + (e >> 1)]; sum += __uint_as_float((e & 1) ? (d & 0xffff0000u) : (d << 16)); }
            r[e] = fb[i][t][e] + om[t] * sum;
          }
          const int tok = 128 * mt + 32 * s + 16 * t + (lane & 15);
          *(u32x2*)(out + (size_t)tok * 1152 + (144 * nt + 16 * j + 4 * (lane >> 4)) * 2) = (u32x2){pk2(r[0], r[1]), pk2(r[2], r[3])};
        }
      }
    }
  }
  if (bid == 0 && wave == 3) {
    const u64* fp = (const u64*)(ws + WS_FLAG) + lane;
    for (unsigned spin = 0; spin < (1u << 14); ++spin) {
      bool okk = true;
#pragma unroll
      for (int i = 0; i < 4; ++i) { const u64 v = __hip_atomic_load(fp + 64 * i, __ATOMIC_RELAXED, __HIP_MEMORY_SCOPE_AGENT); okk &= (unsigned)(v >> 32) == thi && (unsigned)v - tlo - 1u <= 6u; }
      if (__all(okk)) break;
      __builtin_amdgcn_s_sleep(2);
    }
    if (lane == 0) __hip_atomic_store((u64*)(ws + WS_FLAG + 16384), ((u64)thi << 32) | tlo, __ATOMIC_RELAXED, __HIP_MEMORY_SCOPE_AGENT);
  }
  STAMP(10);
}

extern "C" void kernel_launch(void* const* d_in, const int* in_sizes, int n_in, void* d_out, int out_size, void* d_ws, size_t ws_size, hipStream_t stream) {
  (void)hipFuncSetAttribute((const void*)kf, hipFuncAttributeMaxDynamicSharedMemorySize, LDS_BYTES);
  hipLaunchKernelGGL(kf, dim3(256), dim3(256), LDS_BYTES, stream, (const char*)d_in[0], (const char*)d_in[1], (const char*)d_in[2], (const char*)d_in[4],
                     (const char*)d_in[5], (const char*)d_in[6], (const char*)d_in[7], (char*)d_ws, (char*)d_out);
}
```

```cpp
#include <hip/hip_runtime.h>
#include <hip/hip_bf16.h>
#define PF0 2
#include <stdint.h>
#include <utility>
template <int N> struct IC { static constexpr int v = N; };
template <class F, int... Is> __device__ __forceinline__ void sfor_(F&& f, std::integer_sequence<int, Is...>) { (f(IC<Is>{}), ...); }
template <int N, class F> __device__ __forceinline__ void sfor(F&& f) { sfor_(f, std::make_integer_sequence<int, N>{}); }

typedef __bf16 bf16x2 __attribute__((ext_vector_type(2)));
typedef __bf16 bf16x4 __attribute__((ext_vector_type(4)));
typedef __bf16 bf16x8 __attribute__((ext_vector_type(8)));
typedef float f32x2 __attribute__((ext_vector_type(2)));
typedef float f32x4 __attribute__((ext_vector_type(4)));
typedef unsigned u32x2 __attribute__((ext_vector_type(2)));
typedef unsigned u32x4 __attribute__((ext_vector_type(4)));
typedef unsigned long long u64;
#define NTW ""
#define STP " sc1"
#define LAS __attribute__((address_space(3)))
#define DI __device__ __forceinline__
#define SB() __builtin_amdgcn_sched_barrier(0)
#define MFMA16(a, b, c) __builtin_amdgcn_mfma_f32_16x16x32_bf16((a), (b), (c), 0, 0, 0)
#define BAR() do { asm volatile("" ::: "memory"); __builtin_amdgcn_s_barrier(); asm volatile("" ::: "memory"); } while (0)
#define PIN(x) asm volatile("" : "+v"(x))
#define STAMP(i)

constexpr int NJ = 162;
constexpr float LOG2E = 1.4426950408889634f;
constexpr unsigned WS_HF = 0, HF_BYTES = 32u * NJ * 4096u;
constexpr unsigned WS_MAIL = 24u << 20;
constexpr unsigned WS_Z = 40u << 20;
constexpr unsigned WS_FLAG = 41u << 20;
constexpr int NLOC = 17, HXOFF = NLOC * 8192, CTL = HXOFF + 18432, PFOFF = CTL + 256, LDS_BYTES = 163840;
constexpr int AVS = 48, AKX = HXOFF + 272 * AVS;
static_assert(AKX + 4096 + 256 <= CTL, "attention staging");
constexpr int OB0 = 6 * 18432, OB1 = 0, XOFF = 2 * 18432;

DI unsigned pk2(float lo, float hi) { f32x2 v = {lo, hi}; bf16x2 b = __builtin_convertvector(v, bf16x2); return __builtin_bit_cast(unsigned, b); }
DI bf16x4 trhalf(unsigned addr, int off) { return __builtin_amdgcn_ds_read_tr16_b64_v4bf16((LAS bf16x4*)(addr + off)); }
DI bf16x8 cat(bf16x4 a, bf16x4 b) { return __builtin_shufflevector(a, b, 0, 1, 2, 3, 4, 5, 6, 7); }
DI bf16x8 ld128(unsigned addr, int off) { return *(LAS bf16x8*)(addr + off); }
DI u32x4 mksrd(const void* p, unsigned bytes) { return (u32x4){(unsigned)(size_t)p, (unsigned)((size_t)p >> 32) & 0xffffu, bytes, 0x00020000u}; }
DI int lane_id() { int l = __builtin_amdgcn_mbcnt_hi(~0u, __builtin_amdgcn_mbcnt_lo(~0u, 0u)); asm volatile("" : "+v"(l)); return l; }
template <int L, int S> DI void bdma(unsigned lw, unsigned voff, u32x4 srd) {
  unsigned so;
  asm volatile("s_add_u32 m0, %1, %4\n\ts_mov_b32 %0, %5\n\tbuffer_load_dwordx4 %2, %3, %0 offen lds" : "=&s"(so) : "s"(lw), "v"(voff), "s"(srd), "i"(L), "i"(S) : "scc");
}
template <int L, int S> DI void bdma32(unsigned lw, unsigned voff, u32x4 srd) {
  unsigned so;
  asm volatile("s_add_u32 m0, %1, %4\n\ts_mov_b32 %0, %5\n\ts_mov_b32 exec_hi, 0\n\tbuffer_load_dwordx4 %2, %3, %0 offen lds\n\ts_mov_b32 exec_hi, -1" : "=&s"(so) : "s"(lw), "v"(voff), "s"(srd), "i"(L), "i"(S) : "scc");
}
template <int L, int S> DI void bdmar(unsigned lw, unsigned voff, u32x4 srd, unsigned run) {
  unsigned so;
  asm volatile("s_add_u32 m0, %1, %4\n\ts_add_u32 %0, %6, %5\n\tbuffer_load_dwordx4 %2, %3, %0 offen" NTW " lds" : "=&s"(so) : "s"(lw), "v"(voff), "s"(srd), "i"(L), "i"(S), "s"(run) : "scc");
}
template <int L, int S> DI void bdmar32(unsigned lw, unsigned voff, u32x4 srd, unsigned run) {
  unsigned so;
  asm volatile("s_add_u32 m0, %1, %4\n\ts_add_u32 %0, %6, %5\n\ts_mov_b32 exec_hi, 0\n\tbuffer_load_dwordx4 %2, %3, %0 offen" NTW " lds\n\ts_mov_b32 exec_hi, -1" : "=&s"(so) : "s"(lw), "v"(voff), "s"(srd), "i"(L), "i"(S), "s"(run) : "scc");
}
template <int L> DI void vdma(unsigned lw, const void* vaddr) {
  asm volatile("s_add_u32 m0, %0, %2\n\ts_nop 0\n\tglobal_load_lds_dwordx4 %1, off" NTW ::"s"(lw), "v"(vaddr), "i"(L) : "scc");
}
template <int N> DI void waitv() { asm volatile("s_waitcnt vmcnt(%0)" ::"n"(N > 63 ? 63 : N) : "memory"); }
DI void lds_arrive(unsigned addr, unsigned one) {
  asm volatile("s_mov_b64 exec, 1\n\tds_add_u32 %0, %1\n\ts_mov_b64 exec, -1" ::"v"(addr), "v"(one) : "memory");
}
template <int S, int O> DI void st16(unsigned voff, u32x4 srd, u32x4 v) {
  unsigned so;
  asm volatile("s_mov_b32 %0, %4\n\tbuffer_store_dwordx4 %1, %2, %3, %0 offen offset:%5" STP "\n\ts_nop 1" : "=&s"(so) : "v"(v), "v"(voff), "s"(srd), "i"(S), "i"(O) : "memory");
}
template <int O> DI void ld16s(bf16x8& d, unsigned voff, u32x4 srd, unsigned so) {
  asm volatile("buffer_load_dwordx4 %0, %1, %2, %3 offen offset:%4 sc1" : "=v"(d) : "v"(voff), "s"(srd), "s"(so), "i"(O));
}
template <int O> DI void ld8s(u32x2& d, unsigned voff, u32x4 srd, unsigned so) {
  asm volatile("buffer_load_dwordx2 %0, %1, %2, %3 offen offset:%4 sc1" : "=v"(d) : "v"(voff), "s"(srd), "s"(so), "i"(O));
}
template <int O> DI void st16s(unsigned voff, u32x4 srd, unsigned so, u32x4 v) {
  asm volatile("buffer_store_dwordx4 %0, %1, %2, %3 offen offset:%4 sc1\n\ts_nop 1" ::"v"(v), "v"(voff), "s"(srd), "s"(so), "i"(O) : "memory");
}
template <int O> DI void st8s(unsigned voff, u32x4 srd, unsigned so, u32x2 v) {
  asm volatile("buffer_store_dwordx2 %0, %1, %2, %3 offen offset:%4 sc1" ::"v"(v), "v"(voff), "s"(srd), "s"(so), "i"(O) : "memory");
}
template <int O> DI void st4s(unsigned voff, u32x4 srd, unsigned so, unsigned v) {
  asm volatile("buffer_store_dword %0, %1, %2, %3 offen offset:%4 sc1" ::"v"(v), "v"(voff), "s"(srd), "s"(so), "i"(O) : "memory");
}
template <char C, char B, bool Z> DI void mma(f32x4& acc, bf16x8 a, bf16x8 b) {
  if constexpr (C == 'v' && B == 'v' && !Z) asm volatile("v_mfma_f32_16x16x32_bf16 %0, %1, %2, %0" : "+v"(acc) : "v"(a), "v"(b));
  if constexpr (C == 'v' && B == 'v' && Z) asm volatile("v_mfma_f32_16x16x32_bf16 %0, %1, %2, 0" : "=v"(acc) : "v"(a), "v"(b));
  if constexpr (C == 'v' && B == 'a' && !Z) asm volatile("v_mfma_f32_16x16x32_bf16 %0, %1, %2, %0" : "+v"(acc) : "v"(a), "a"(b));
  if constexpr (C == 'v' && B == 'a' && Z) asm volatile("v_mfma_f32_16x16x32_bf16 %0, %1, %2, 0" : "=v"(acc) : "v"(a), "a"(b));
  if constexpr (C == 'a' && B == 'v' && !Z) asm volatile("v_mfma_f32_16x16x32_bf16 %0, %1, %2, %0" : "+a"(acc) : "v"(a), "v"(b));
  if constexpr (C == 'a' && B == 'a' && !Z) asm volatile("v_mfma_f32_16x16x32_bf16 %0, %1, %2, %0" : "+a"(acc) : "v"(a), "a"(b));
  if constexpr (C == 'a' && B == 'a' && Z) asm volatile("v_mfma_f32_16x16x32_bf16 %0, %1, %2, 0" : "=&a"(acc) : "v"(a), "a"(b));
}
DI void fix(bf16x8& a, bf16x8& b) {
  u32x4 x = __builtin_bit_cast(u32x4, a), y = __builtin_bit_cast(u32x4, b);
  asm volatile("" : "+v"(x), "+v"(y));
  unsigned x2 = x[2], x3 = x[3], y0 = y[0], y1 = y[1];
  asm volatile("v_swap_b32 %0, %2\n\tv_swap_b32 %1, %3\n\ts_nop 1" : "+v"(x2), "+v"(x3), "+v"(y0), "+v"(y1));
  x[2] = x2; x[3] = x3; y[0] = y0; y[1] = y1;
  a = __builtin_bit_cast(bf16x8, x); b = __builtin_bit_cast(bf16x8, y);
}
DI int kap(int rho) { return 8 * ((rho >> 2) & 3) + 4 * (rho >> 4) + (rho & 3); }
DI float red_max(float v) {
  auto a = __builtin_amdgcn_permlane16_swap(__float_as_uint(v), __float_as_uint(v), false, false);
  v = fmaxf(__uint_as_float(a[0]), __uint_as_float(a[1]));
  auto b = __builtin_amdgcn_permlane32_swap(__float_as_uint(v), __float_as_uint(v), false, false);
  return fmaxf(__uint_as_float(b[0]), __uint_as_float(b[1]));
}
DI float red_sum(float v) {
  auto a = __builtin_amdgcn_permlane16_swap(__float_as_uint(v), __float_as_uint(v), false, false);
  v = __uint_as_float(a[0]) + __uint_as_float(a[1]);
  auto b = __builtin_amdgcn_permlane32_swap(__float_as_uint(v), __float_as_uint(v), false, false);
  return __uint_as_float(b[0]) + __uint_as_float(b[1]);
}

#define PFST 13
constexpr int PD = 3;
constexpr int NS1 = 24, NG = 18, NST = NS1 + NG, NB1 = 54;
constexpr int LDH = 6;
enum { K_NORM = 0, K_SEL = 1, K_HALO = 2, K_G = 3 };
enum { V_NONE = 0, V_WB, V_XP, V_HX, V_ST, V_GW, V_GH, V_PL, V_PF, V_OW, V_OH };
enum { T_NONE = 0, T_EA, T_SIG, T_EB, T_EQ, T_ATT };
struct Unit { int stage, blk, ks, base, slot, kind, koff; };
constexpr int MAXU = 640;
struct Prog {
  int nu; Unit u[MAXU];
  int sfirst[NST + 1];
  int bstage[NB1 + NG], boff[NB1 + NG], biss[NB1 + NG];
  int vm[MAXU][4], va[MAXU][4];
  int ft[MAXU][4], fo[MAXU][4];
  int nwait[NST], nfirst;
  int pub[NST];
  int chk[MAXU];
  int chkw[MAXU];
  int tailops;
  bool ok;
};
constexpr int w0(const Prog& P, int n) { const int U = P.sfirst[n + 1] - P.sfirst[n]; return P.sfirst[n] + (U / 2 < U - PD ? U / 2 : U - PD); }
constexpr int ATT_PREP = 3, ATT_TILE = 12;
constexpr int ATT0 = 4 * ATT_PREP + 3, NATT = ATT0 + 4 * ATT_TILE + 1;
constexpr bool op_is_store(int task, int o) {
  if (task == T_EA) return o % 31 == 30;
  if (task == T_EB) return o % 7 == 6;
  if (task == T_EQ) return o % 31 == 30;
  if (task == T_ATT) { if (o < ATT0) return o == 4 * ATT_PREP; if (o == NATT - 1) return true; return (o - ATT0) % (2 * ATT_TILE) == 2 * ATT_TILE - 1; }
  return false;
}
constexpr Prog make_prog() {
  Prog P{}; P.ok = true;
  int nu = 0;
  auto add = [&](int st, int blk, int ks, int base, int slot, int kind, int koff) { P.u[nu] = Unit{st, blk, ks, base, slot, kind, koff}; ++nu; };
  for (int n = 0; n < 9; ++n) {
    P.sfirst[n] = nu;
    for (int u = 0; u < 16; ++u) { const int hp = u >> 3, kk = (u >> 2) & 1, pb = (u >> 1) & 1, isU = u & 1; add(n, 2 * n + isU, 2 * n + kk, 2 * hp + pb, 4 * hp + 2 * pb + isU, K_NORM, kk * 4096); }
    P.bstage[2 * n] = P.bstage[2 * n + 1] = n;
  }
  for (int n = 9; n < 15; ++n) {
    P.sfirst[n] = nu;
    const int e = (n - 9) % 3, fb = 18 + 3 * (n - 9), s0 = n < 12 ? 8 : 12;
    const bool skew = n == 9 || n == 14;
    for (int u = 0; u < 24; ++u) {
      const int ks = skew ? (u % 12) >> 1 : u >> 2, tile = skew ? 2 * (u / 12) + (u & 1) : u & 3;
      add(n, fb + (ks >> 1), 6 * e + ks, tile, s0 + tile, K_NORM, (ks & 1) * 4096);
    }
    for (int i = 0; i < 3; ++i) P.bstage[fb + i] = n;
  }
  for (int n = 15; n < 24; ++n) {
    P.sfirst[n] = nu;
    for (int kk = 0; kk < 2; ++kk) {
      const int ks = 2 * (n - 15) + kk, b = 36 + ks;
      add(n, b, ks, 4, 0, K_NORM, 0); add(n, b, ks, 5, 1, K_NORM, 0); add(n, b, ks, 6, 2, K_NORM, 0);
      add(n, b, ks, 8, 4, K_NORM, 0); add(n, b, ks, 9, 5, K_NORM, 0);
      add(n, b, ks, 7, 3, K_SEL, 0); add(n, b, ks, 10, 6, K_HALO, 0);
      P.bstage[b] = n;
    }
  }
  for (int i = 0; i < NG; ++i) {
    const int n = NS1 + i; P.sfirst[n] = nu;
    for (int j = 0; j < 9; ++j) add(n, NB1 + i, i, 11, j, K_G, 32 * j);
    P.bstage[NB1 + i] = n;
  }
  P.sfirst[NST] = nu; P.nu = nu;
  int freeat[NLOC] = {}; int cnt[NST + 1] = {};
  for (int l = 0; l < NLOC; ++l) freeat[l] = l < 9 ? -1 : 8;
  int prev = -1;
  for (int b = 0; b < NB1; ++b) {
    const int l = b <= 24 ? (b + 2) % 9 : (b - 16) % 17;
    int is = freeat[l] > prev ? freeat[l] : prev;
    if (b == 25) is = 8;
    while (is >= 0 && cnt[is] >= (is < 8 ? 2 : (is == 8 ? 4 : (is < 15 ? 6 : 3)))) ++is;
    if (is >= 0) ++cnt[is];
    P.boff[b] = l * 8192; P.biss[b] = is; prev = is; freeat[l] = P.bstage[b] + 1;
  }
  prev = 16;
  for (int i = 0; i < NG; ++i) {
    const int b = NB1 + i, off = ((i + 2) % 7) * 18432; int fr = prev;
    if (i < 7) { for (int l = off / 8192; l <= (off + 18431) / 8192; ++l) if (freeat[l] > fr) fr = freeat[l]; }
    else if (P.bstage[b - 7] + 1 > fr) fr = P.bstage[b - 7] + 1;
    P.boff[b] = off; P.biss[b] = fr; prev = fr;
    if (fr + 2 > P.bstage[b]) P.ok = false;
  }
  struct Op { int e, kind, arg; };
  Op sw[400] = {}, sx[200] = {}; int nw = 0, nx = 0;
  for (int b = 4; b < NB1; ++b) for (int h = 0; h < 2; ++h) sw[nw++] = Op{P.biss[b] < 0 ? 0 : w0(P, P.biss[b]), V_WB, 2 * b + h};
  for (int i = 0; i < NG; ++i) for (int p = 0; p < 5; ++p) sw[nw++] = Op{w0(P, P.biss[NB1 + i]), V_GW, 8 * i + p};
  for (int st = 0; st < 2; ++st) for (int p = 0; p < 10; ++p) sw[nw++] = Op{w0(P, NS1 + 12 + 2 * st), V_OW, 16 * st + p};
  for (int d = 2; d < 9; ++d) for (int i = 0; i < 8; ++i) sx[nx++] = Op{d == 2 ? 1 : P.sfirst[d - 3] + 9, V_XP, 8 * d + i};
  for (int i = 0; i < 5; ++i) sx[nx++] = Op{P.sfirst[7], V_HX, i};
  for (int i = PF0; i < 3; ++i) sx[nx++] = Op{P.sfirst[PFST] + 2 + 4 * i, V_PF, i};
  const int ck[5] = {0, NS1 - LDH, NS1 + 8 - LDH, NS1 + 13, NST};
  for (int n = NS1 - LDH - 2; n < NST; ++n) {
    for (int p = 1; p <= 4; ++p) {
      if (n == ck[p] - (p == 2 ? 3 : 2)) sx[nx++] = Op{P.sfirst[n], V_PL, p};
      if (n == ck[p]) P.chk[P.sfirst[n]] = p;
      if (p == 4) P.chk[nu] = 4;
    }
    for (int i = 0; i < NG; ++i) if (NS1 + i - (i >= 16 ? 3 : LDH) == n) for (int q = 0; q < 4; ++q) sx[nx++] = Op{P.sfirst[n], V_GH, 4 * i + q};
  }
  {
    int iw = 0, ix = 0; bool lastw = false;
    for (int U = 0; U < nu; ++U) {
      const int n = P.u[U].stage, lu = U - P.sfirst[n];
      if (P.u[U].kind == K_HALO || (n < 8 && lu >= 2 && lu < 6)) continue;
      const int per = (n < 2 || n == 8 || n == 9 || n == NS1 || n == NS1 + 1) ? 2 : 1;
      for (int k = 0; k < per; ++k) {
        const bool cw = iw < nw && sw[iw].e <= U, cx = ix < nx && sx[ix].e <= U;
        if (!cw && !cx) break;
        const bool usew = cw && (!cx || !lastw);
        const Op o = usew ? sw[iw++] : sx[ix++]; lastw = usew;
        const int t = per == 2 ? 1 + 2 * k : 3;
        P.vm[U][t] = o.kind; P.va[U][t] = o.arg;
      }
    }
    if (iw < nw || ix < nx) P.ok = false;
  }
  auto task = [&](int id, int nops, int U, int tmin, int o0 = 0) -> int {
    int t = 0;
    for (int o = o0; o < nops; ++o) {
      for (;;) {
        if (t < tmin) t = tmin;
        const bool halo = P.u[U].kind == K_HALO;
        const bool xrd = P.u[U].stage < 8 && U - P.sfirst[P.u[U].stage] >= 2 && U - P.sfirst[P.u[U].stage] < 6;
        if (!halo && !xrd && P.vm[U][t] == V_NONE && P.ft[U][t] == T_NONE) break;
        ++t; if (t > 3 || halo) { t = 0; ++U; }
      }
      P.ft[U][t] = id; P.fo[U][t] = o;
      if (op_is_store(id, o)) P.vm[U][t] = V_ST;
    }
    return U;
  };
  int e = 0;
  e = task(T_EA, 62, P.sfirst[8] + 9, 0);               if (e >= P.sfirst[9] + 12) P.ok = false;
  e = task(T_EA, 248, e + 1, 0, 62);                    if (e >= P.sfirst[12]) P.ok = false;
  const int endA = e;
  e = task(T_SIG, 96, P.sfirst[12] + 1, 1);             if (e >= P.sfirst[14] + 12) P.ok = false;
  const int es = e;
  e = task(T_EB, 28, P.sfirst[14] + 13, 0);             if (e >= P.sfirst[15]) P.ok = false;
  e = task(T_SIG, 192, es + 1, 0, 96);                  if (e >= P.sfirst[15]) P.ok = false;
  e = task(T_EB, 56, P.sfirst[15] + 1, 0, 28);
  const int endB = e;
  e = task(T_EQ, 62, P.sfirst[NS1] + 1, 0);
  const int endQ = e;
  e = task(T_ATT, ATT0 - 1, (e + 1 > w0(P, NS1) + 1 ? e + 1 : w0(P, NS1) + 1), 0);
  int sa = P.u[e].stage + (e == P.sfirst[P.u[e].stage] ? 0 : 1);
  e = task(T_ATT, NATT, w0(P, sa) + 1, 1, ATT0 - 1);
  if (P.u[e].stage >= NS1 + 11) P.ok = false;
  const int endO = e;
  int pos = 0; int posW[NB1 + NG] = {}, posX[9] = {}, posH[NG] = {}, posPL[5] = {}, poshx = -1, posU[MAXU + 1] = {}, posOW0 = -1;
  pos += 2; posW[0] = pos - 1; pos += 2; posW[1] = pos - 1; pos += 8; posX[0] = pos - 1;
  pos += 2; posW[2] = pos - 1; pos += 2; posW[3] = pos - 1; pos += 8; posX[1] = pos - 1;
  P.nfirst = pos - 1 - posX[0];
  for (int U = 0; U < nu; ++U) {
    posU[U] = pos;
    for (int t = 0; t < 4; ++t) {
      const int k = P.vm[U][t], a = P.va[U][t];
      if (k == V_NONE) continue;
      if (k == V_WB) posW[a >> 1] = pos;
      if (k == V_XP) posX[a >> 3] = pos;
      if (k == V_HX) poshx = pos;
      if (k == V_GW) posW[NB1 + (a >> 3)] = pos;
      if (k == V_GH) posH[a >> 2] = pos;
      if (k == V_PL) posPL[a] = pos;
      ++pos;
    }
  }
  posU[nu] = pos;
  int covered[NST] = {};
  for (int n = 0; n + 1 < NST; ++n) {
    int last = -1;
    for (int b = 0; b < NB1 + NG; ++b) if (P.bstage[b] == n + 1 && posW[b] > last) last = posW[b];
    if (n + 1 < 9 && posX[n + 1] > last) last = posX[n + 1];
    if (n + 1 == 15 && poshx > last) last = poshx;
    if (n + 1 >= NS1 && posH[n + 1 - NS1] > last) last = posH[n + 1 - NS1];
    int nw = posU[P.sfirst[n] + 1] - 1 - last;
    if (nw < 0) P.ok = false;
    P.nwait[n] = nw; covered[n] = last;
  }
  const int ends[5] = {0, endA, endB, endQ, endO};
  for (int lev = 1; lev <= 4; ++lev) {
    const int lastop = posU[ends[lev] + 1] - 1;
    int n = P.u[ends[lev]].stage + (lev == 1 ? 3 : (lev == 2 ? 4 : 5));
    for (int q = P.u[ends[lev]].stage + 1; q < n; ++q) if (covered[q] >= lastop) { n = q; break; }
    if (n + 1 >= NST) { P.ok = false; break; }
    if (covered[n] < lastop) { covered[n] = lastop; P.nwait[n] = posU[P.sfirst[n] + 1] - 1 - lastop; }
    if (P.pub[n] < lev) P.pub[n] = lev;
  }
  for (int U = 0; U <= nu; ++U) if (P.chk[U]) { P.chkw[U] = posU[U] - 1 - posPL[P.chk[U]]; if (P.chkw[U] < 0) P.ok = false; }
  (void)posOW0;
  return P;
}
constexpr Prog PG = make_prog();
static_assert(PG.ok, "schedule");
constexpr int rd_abs(int U) { return PG.boff[PG.u[U].blk] + PG.u[U].koff; }
constexpr int rd_win(int U) { return PG.u[U].kind == K_G ? (rd_abs(U) >= 73728 ? 73728 : 0) : (rd_abs(U) >> 16) << 16; }
constexpr int rd_prevwin(int U) { for (int q = U - 1; q >= 0; --q) if (PG.u[q].base == PG.u[U].base) return rd_win(q); return 0; }
constexpr int rd_h(int U) { return PG.u[U].kind == K_G ? 4608 : (PG.u[U].base < 4 ? 2048 : 4096); }
constexpr int g_j(int i) { return i < 16 ? 18 * ((i & 7) >> 2) + 4 * (i & 3) + 2 * (i >> 3) : 18 * (i - 16) + 16; }

__global__ __launch_bounds__(256, 1) void kf(const char* __restrict__ x, const char* __restrict__ wqkv, const char* __restrict__ wo, const char* __restrict__ wg,
                                              const char* __restrict__ wu, const char* __restrict__ wd, const char* __restrict__ gw, char* __restrict__ ws, char* __restrict__ out) {
  extern __shared__ __attribute__((aligned(16))) char smem[];
  const int tid = threadIdx.x; const int lane0 = tid & 63;
  const int wave = __builtin_amdgcn_readfirstlane(tid >> 6);
  const int bid = blockIdx.x, xcd = bid & 7, idx = bid >> 3;
  const int m = 4 * (xcd & 1) + (idx & 3), c = 8 * (xcd >> 1) + (idx >> 2), gam = c >> 2, ci = c & 3;
  const int s = xcd >> 1, mt = 8 * (xcd & 1) + (idx & 7), nt = idx >> 3;
  const int tg2 = wave & 1, kh = wave >> 1;
  const unsigned lbase = (unsigned)(size_t)smem;
  STAMP(0);
  u32x4 rc;
  {
    u64 pc; unsigned csz;
    asm volatile("s_getpc_b64 %0\n.Lkf_code_begin:\n\ts_mov_b32 %1, .Lkf_code_end-.Lkf_code_begin" : "=s"(pc), "=s"(csz));
    rc = mksrd((const void*)pc, csz);
  }
  const unsigned vcode = ((lane0 & 7) * 128 + idx * 4 + wave) * 128;
  asm volatile("s_mov_b32 m0, %0\n\ts_nop 0\n\tbuffer_load_dword %1, %2, 0 offen lds" ::"s"(lbase + PFOFF + 256 * wave), "v"(vcode), "s"(rc));
  const unsigned vx = (lane0 >> 3) * 1152 + (((lane0 & 7) ^ (lane0 >> 3)) << 4);
  const unsigned vw = kap(8 * wave + (lane0 >> 3)) * 9216 + (((((lane0 >> 1) & 3) ^ (lane0 >> 4)) << 1 | (lane0 & 1)) << 4);
  const unsigned wcol = 1152 * gam + 256 * ci;
  const u32x4 rx = mksrd(x + (size_t)(256 * m + 64 * wave) * 1152, 64 * 1152);
  const u32x4 rg = mksrd(wg + wcol, 576 * 9216 - wcol), ru = mksrd(wu + wcol, 576 * 9216 - wcol);
  const unsigned lw = lbase + 1024 * wave, lx = lbase + 9 * 8192 + 16384 * wave;
  const char* pq = nullptr; unsigned pqs = 0;
#define ROT1 ((2 * (m & 3) + (c & 7)) % 9)
  const unsigned rot = ROT1;
  unsigned runw = rot, runx = rot;
  auto adv = [&](unsigned& r) { r = r == 8 ? 0 : r + 1; asm volatile("" : "+s"(r)); };
  auto wblock = [&](auto A) {
    constexpr int b = decltype(A)::v >> 1, h2 = decltype(A)::v & 1, L = PG.boff[b] + h2 * 4096;
    if constexpr (b < 36) {
      const unsigned run = runw * (64 * 9216);
      if constexpr (b < 18) bdmar<L, h2 * (32 * 9216)>(lw, vw, (b & 1) ? ru : rg, run);
      else bdmar<L, 128 + h2 * (32 * 9216)>(lw, vw, b < 27 ? rg : ru, run);
      if constexpr (h2 == 1 && (b >= 18 || (b & 1))) adv(runw);
    } else {
      vdma<L>(lw, pq + (size_t)pqs * (runw * 16 + (b & 1) * 8 + h2));
      if constexpr (h2 == 1 && (b & 1)) adv(runw);
    }
  };
  auto xpiece = [&](auto A) { constexpr int d = decltype(A)::v >> 3, i = decltype(A)::v & 7; bdmar<(d & 1) * 8192 + 1024 * i, 8 * 1152 * i>(lx, vx, rx, runx * 128); if constexpr (i == 7) adv(runx); };
  sfor<4>(wblock);
  sfor<8>(xpiece);
  sfor<4>([&](auto A) { wblock(IC<4 + A.v>{}); });
  sfor<8>([&](auto A) { xpiece(IC<8 + A.v>{}); });
  if (tid < 64) *(LAS unsigned*)(lbase + CTL + 4 * lane0) = 0u;
  u64 tag = *(const __attribute__((address_space(4))) u64*)(size_t)(ws + WS_FLAG + 16384);
  tag = (tag ^ (tag >> 29)) * 0x9E3779B97F4A7C15ull + 0x632BE59BD9B4E019ull; tag ^= tag >> 32; tag *= 0xD6E8FEB86659FD93ull; tag &= ~7ull;
  const unsigned tlo = __builtin_amdgcn_readfirstlane((unsigned)tag), thi = __builtin_amdgcn_readfirstlane((unsigned)(tag >> 32));

  const unsigned axr = lx + (lane0 & 15) * 128 + (((lane0 >> 4) ^ (lane0 & 7)) << 4);
  unsigned acnt = lbase + CTL, one = 1u; PIN(acnt); PIN(one);
  const unsigned vh = lane0 * 16;
  const u32x4 rh = mksrd(ws + WS_HF, HF_BYTES);
  const u32x4 rhs = mksrd(ws + WS_HF + (size_t)((4 * m + wave) * NJ + 18 * gam + 4 * ci) * 4096, 4 * 4096);
  const u32x4 rf = mksrd(ws + WS_FLAG, 32768);
  unsigned bs[12], vg[5], ahx, av, avw, avr, avl, pmask, vo2, vpf[3], vpl8, vpl32;
  bool bok[4], bodd, bg0, brow0; const char* phx;
  {
    const int l2 = lane0, tau = l2 & 15, g = l2 >> 4, q4 = tau >> 2, p4 = l2 & 3;
    {
      const int r = 8 * (wave & 1) + (l2 >> 3); int tok = 256 * m - 16 + r; if (tok < 0) tok = 0;
      phx = (r == 0 ? gw : x + (size_t)tok * 1152) + (((l2 & 7) ^ (l2 >> 3)) << 4); PIN(phx);
    }
    const unsigned aw0 = lbase + (4 * g + q4) * 128 + (((2 * g + (q4 >> 1)) & 3) << 5) + 8 * p4;
#pragma unroll
    for (int i = 0; i < 4; ++i) bs[i] = aw0 ^ (i << 5);
    {
      const int row = 4 * wave + g, uq = (tau >> 1) ^ (row & 7), ch = (uq << 1) | (tau & 1), kr = kap(row);
      const bool gu = ch >= 6 && ch < 10;
      const int seg = ch < 6 ? ch >> 1 : (ch < 13 ? ch - 10 : 2);
      pqs = gu ? 4 * 9216 : 4 * 3456;
      pq = (gu ? (ch < 8 ? wg : wu) : wqkv) + (kr * (pqs >> 2) + (gu ? 1152 * gam + 1024 + 32 * ci + 16 * (ch & 1) : 1152 * seg + 36 * c + (ch < 6 ? 16 * (ch & 1) : 20)));
      PIN(pq); PIN(pqs);
    }
    const int rr = 4 * g + q4;
    const unsigned aq0 = lbase + rr * 256 + ((rr & 7) << 5) + 8 * p4;
    const unsigned aq3 = lbase + rr * 256 + (((p4 < 2 ? 5 : 6) ^ (rr & 7)) << 5) + ((p4 & 1) ? 24 : 8);
    bs[4] = aq0; bs[5] = aq0 ^ 32; bs[6] = aq0 ^ 64; bs[7] = aq3; bs[8] = aq0 ^ 96; bs[9] = aq0 ^ 128;
    bs[10] = wave == 0 ? bs[5] : (wave == 1 ? bs[6] : aq3);
    bs[11] = lbase + kh * 9216 + rr * 288 + 8 * p4;
#pragma unroll
    for (int i = 0; i < 12; ++i) PIN(bs[i]);
    ahx = lbase + HXOFF + tau * 128 + ((g ^ (l2 & 7)) << 4); PIN(ahx);
#pragma unroll
    for (int i = 0; i < 5; ++i) { const int q = 64 * i + (i == 4 ? (l2 & 31) : l2); vg[i] = (q / 18) * 1152 + (q % 18) * 16; PIN(vg[i]); }
    av = lbase + HXOFF + (16 + 64 * wave + tau) * AVS + 8 * g;
    avw = g == 2 ? lbase + HXOFF + (16 + 64 * wave + tau) * AVS + 32 : lbase + PFOFF + 1024 + 4 * l2;
    avl = lbase + HXOFF + (64 * wave + rr) * AVS + 32; avr = avl - 32 + 8 * p4;
    pmask = (tau & 1) ? 0xffff0000u : 0xffffu;
    vo2 = (g >> 1) * 128 + tau * 8 + (g & 1) * 4;
    const int qq = idx * 4 + wave;
    vpf[0] = (qq * 81 + l2) * 128; vpf[1] = vpf[0] + 17 * 128; vpf[2] = (qq * 41 + (l2 < 41 ? l2 : 40)) * 128;
    vpl8 = ((mt >> 1) * 32 + 8 * s + (l2 & 7)) * 8; vpl32 = ((mt >> 1) * 32 + (l2 & 31)) * 8;
    PIN(av); PIN(avw); PIN(avl); PIN(avr); PIN(pmask); PIN(vo2); PIN(vpf[0]); PIN(vpf[2]); PIN(vpl8); PIN(vpl32);
    bodd = tau & 1; bg0 = g == 0; brow0 = tau == 0;
#pragma unroll
    for (int i = 0; i < 4; ++i) bok[i] = (unsigned)(tau + 16 - (16 * (i >> 1) + 4 * g + 2 * (i & 1) + (tau & 1))) <= 14u;
  }
  u64 mk[7] = {__ballot(bok[0]), __ballot(bok[1]), __ballot(bok[2]), __ballot(bok[3]), __ballot(bodd), __ballot(bg0), __ballot(brow0)};
  auto sel = [&](u64 mask, unsigned a1, unsigned a0_) -> unsigned { unsigned r; asm volatile("v_cndmask_b32 %0, %1, %2, %3" : "=v"(r) : "v"(a0_), "v"(a1), "s"(mask)); return r; };
  const unsigned od = (unsigned)(36 * s + kh) * (32 * 1152) + 16 * tg2 * 1152 + 288 * nt;
  u32x4 rd = mksrd(wd + od, 4608 * 1152 - od);
  const u32x4 rgh = mksrd(ws + WS_HF + (size_t)((2 * mt + tg2) * NJ + 36 * s + kh) * 4096, 36 * 4096);
  const unsigned oo = (unsigned)(36 * wave) * 1152 + 288 * nt;
  u32x4 ro = mksrd(wo + oo, 576 * 1152 - oo);
  asm volatile("" : "+s"(rd), "+s"(ro), "+s"(wo), "+s"(wd), "+s"(out));
  bf16x8 xf[18][4], hr[NG][4], of[5][2], xh, xhn;
  f32x4 acc[16][4], acch, ga[9][4];
  u32x4 hw = {0u, 0u, 0u, 0u};
  u32x2 pl[5]; f32x2 zv;
  const f32x4 zero = {0.f, 0.f, 0.f, 0.f};

#define ROT2 (2 * nt + mt)
  const unsigned rot2 = ROT2;
  unsigned gwo = 0, gho = 0;
  auto gj = [&](int i) -> unsigned {
    if (i < 16) { const unsigned p = (i + rot2) & 7; return 18 * (p >> 2) + 4 * (p & 3) + 2 * (i >> 3); }
    return 18 * ((i + rot2) & 1) + 16;
  };
  auto oblock = [&](auto ST, auto PP, auto LL, unsigned ldst) {
    constexpr int st = decltype(ST)::v, h2 = decltype(PP)::v / 5, i = decltype(PP)::v % 5, L = decltype(LL)::v + h2 * 4608 + 1024 * i, S = (144 * st + 18 * h2) * 1152;
    if constexpr (i < 4) bdma<L, S>(ldst, vg[i], ro); else bdma32<L, S>(ldst, vg[4], ro);
  };
  auto whalf = [&](auto UU, int h) -> bf16x4 {
    constexpr int U = decltype(UU)::v, bi = PG.u[U].base, win = rd_win(U), pw = rd_prevwin(U);
    if (h == 0 && win != pw) { bs[bi] += (unsigned)(win - pw); PIN(bs[bi]); }
    return trhalf(bs[bi], rd_abs(U) - win + h * rd_h(U));
  };
  const float nl2e = -LOG2E;
  auto eop = [&](f32x4& gq, f32x4& uq, int op, int half) {
    if (op < 2 || (op >= 6 && op < 8) || (op >= 12 && op < 14)) {
      const int i = 2 * (op & 1); float a = gq[i], b = gq[i + 1];
      if (op < 2) asm volatile("v_mul_f32 %0, %2, %0\n\tv_mul_f32 %1, %2, %1" : "+v"(a), "+v"(b) : "s"(nl2e));
      else if (op < 8) asm volatile("v_add_f32 %0, 1.0, %0\n\tv_add_f32 %1, 1.0, %1" : "+v"(a), "+v"(b));
      else asm volatile("v_mul_f32 %0, %2, %0\n\tv_mul_f32 %1, %3, %1" : "+v"(a), "+v"(b) : "v"(uq[i]), "v"(uq[i + 1]));
      gq[i] = a; gq[i + 1] = b;
    } else if (op < 12) {
      const int i = op < 6 ? op - 2 : op - 8; float a = gq[i];
      if (op < 6) asm volatile("v_exp_f32 %0, %0" : "+v"(a)); else asm volatile("v_rcp_f32 %0, %0" : "+v"(a));
      gq[i] = a;
    } else {
      unsigned a, b;
      asm volatile("v_cvt_pk_bf16_f32 %0, %2, %3\n\tv_cvt_pk_bf16_f32 %1, %4, %5" : "=&v"(a), "=v"(b) : "v"(gq[0]), "v"(gq[1]), "v"(gq[2]), "v"(gq[3]));
      hw[2 * half] = a; hw[2 * half + 1] = b;
    }
  };
  u32x4 qf[4], kf[4], kp; bf16x8 vf0, vf1; f32x4 s0, s1, o0, o1; float cv[4], mx, sm; u32x4 pw; unsigned ol = 0;

  auto check = [&](auto UU) {
    constexpr int U = decltype(UU)::v, p = PG.chk[U];
    asm volatile("s_waitcnt vmcnt(%1)" : "+v"(pl[p]) : "n"(PG.chkw[U] > 63 ? 63 : PG.chkw[U]) : "memory");
    if (__builtin_expect(!__all(pl[p][1] == thi && pl[p][0] - tlo - p <= 7u - p), 0)) {
      const u64* fp = (const u64*)(ws + WS_FLAG + (p == 4 ? vpl32 : vpl8));
      for (unsigned spin = 0; spin < (1u << 14); ++spin) {
        const u64 v = __hip_atomic_load(fp, __ATOMIC_RELAXED, __HIP_MEMORY_SCOPE_AGENT);
        if (__all((unsigned)(v >> 32) == thi && (unsigned)v - tlo - p <= 7u - p)) break;
        __builtin_amdgcn_s_sleep(2);
      }
    }
    asm volatile("" ::: "memory");
  };
  waitv<PG.nfirst>();
  asm volatile("s_waitcnt lgkmcnt(0)" ::: "memory");
  BAR();
  STAMP(1);
#pragma unroll
  for (int i = 0; i < 8; ++i) xf[i >> 2][i & 3] = ld128(axr ^ ((i >> 2) << 6), (i & 3) * 2048);
  bf16x4 w[PG.nu + PD][2];
  sfor<PD>([&](auto J) { w[J.v][0] = whalf(J, 0); w[J.v][1] = whalf(J, 1); });
  unsigned pv = 0;

  sfor<PG.nu>([&](auto UU) {
    constexpr int U = decltype(UU)::v; constexpr Unit un = PG.u[U];
    constexpr int n = un.stage, lu = U - PG.sfirst[n], W0 = w0(PG, n);
    if constexpr (lu == 0 && n == 9) STAMP(2);
    if constexpr (lu == 0 && n == 12) STAMP(11);
    if constexpr (lu == 0 && n == NS1 + 9) STAMP(12);
    if constexpr (lu == 0 && n == 15) STAMP(3);
    if constexpr (lu == 0 && n == NS1) STAMP(4);
    if constexpr (lu == 1 && n + 1 < NST) { waitv<PG.nwait[n]>(); lds_arrive(acnt, one); }
    if constexpr (U == W0 - 1 && n + 1 < NST) pv = *(volatile LAS unsigned*)(acnt);
    if constexpr (U == W0 && n + 1 < NST) {
      if (__builtin_expect((unsigned)__builtin_amdgcn_readfirstlane(pv) < 4u * (n + 1), 0)) {
        do { pv = *(volatile LAS unsigned*)(acnt); } while ((unsigned)__builtin_amdgcn_readfirstlane(pv) < 4u * (n + 1));
      }
      asm volatile("" ::: "memory");
      if constexpr (PG.pub[n] != 0) {
        if (tid == 0) __hip_atomic_store((u64*)(ws + WS_FLAG) + (m * 32 + c), ((u64)thi << 32) | (tlo + PG.pub[n]), __ATOMIC_RELAXED, __HIP_MEMORY_SCOPE_AGENT);
      }
    }
    if constexpr (PG.chk[U] != 0) check(UU);
    if constexpr (un.kind == K_G && lu == 0 && un.ks < 16) asm volatile("" : "+a"(hr[un.ks][0]), "+a"(hr[un.ks][1]), "+a"(hr[un.ks][2]), "+a"(hr[un.ks][3]));
    if constexpr (un.kind == K_G && lu == 3 && un.ks >= 15 && un.ks < 17) { fix(hr[un.ks + 1][0], hr[un.ks + 1][1]); fix(hr[un.ks + 1][2], hr[un.ks + 1][3]); }
    SB();
    bf16x8 a = cat(w[U][0], w[U][1]);
    if constexpr (un.kind == K_SEL) {
      u32x4 avv = __builtin_bit_cast(u32x4, a), hv = __builtin_bit_cast(u32x4, xh);
#pragma unroll
      for (int e = 0; e < 4; ++e) avv[e] = sel(mk[6], hv[e], avv[e]);
      a = __builtin_bit_cast(bf16x8, avv);
      asm volatile("s_nop 1" : "+v"(a));
    }
    constexpr int UN = U + PD;
    if constexpr (un.kind == K_HALO) {
      mma<'v', 'v', un.ks == 0>(acch, a, xh);
      SB();
      w[UN][0] = whalf(IC<UN>{}, 0); w[UN][1] = whalf(IC<UN>{}, 1);
      xh = xhn;
      SB();
    } else {
      sfor<4>([&](auto T) {
        constexpr int t = decltype(T)::v;
        if constexpr (un.kind == K_G) mma<(t < 2 ? 'v' : 'a'), (un.ks < 16 ? 'a' : 'v'), un.ks == 0>(ga[un.slot][t], a, hr[un.ks][t]);
        else mma<'v', (un.ks < 16 ? 'a' : 'v'), un.ks == 0>(acc[un.slot][t], a, xf[un.ks][t]);
        SB();
        if constexpr (t < 2 && UN < PG.nu) w[UN][t] = whalf(IC<(UN < PG.nu ? UN : 0)>{}, t);
        if constexpr (t >= 2 && n < 8 && lu >= 2 && lu < 6) xf[2 * n + t][lu - 2] = ld128(axr ^ ((t - 2) << 6), ((n + 1) & 1) * 8192 + (lu - 2) * 2048);
        if constexpr (t == 2 && n == 14 && lu == 20) xh = ld128(ahx, 0);
        if constexpr (t == 2 && n >= 15 && n < NS1 && un.base == 8 && un.ks < 17) xhn = ld128(ahx ^ (((un.ks + 1) & 1) << 6), ((un.ks + 1) >> 1) * 2048);
                constexpr int vk = PG.vm[U][t], va = PG.va[U][t];
        if constexpr (vk == V_WB) wblock(IC<va>{});
        if constexpr (vk == V_XP) xpiece(IC<va>{});
        if constexpr (vk == V_HX) {
          int pc = wave + 4 * va; if (pc >= 18) pc -= 4;
          unsigned dd = (pc >> 1) + rot; if (dd >= 9) dd -= 9;
          vdma<HXOFF>(lbase + pc * 1024, phx + 128 * dd);
        }
        if constexpr (vk == V_PF) {
          const u32x4 rp = va < 2 ? mksrd(wd + (size_t)s * (1152 * 1152) + va * (17 * 128), 1152 * 1152 - va * (17 * 128)) : mksrd(wo, 576 * 1152);
          asm volatile("s_mov_b32 m0, %0\n\ts_nop 0\n\tbuffer_load_dword %1, %2, 0 offen lds" ::"s"(lbase + PFOFF + 256 * wave), "v"(vpf[va < 2 ? 0 : 2]), "s"(rp));
        }
        if constexpr (vk == V_GW) {
          constexpr int i = va >> 3, p = va & 7, L = PG.boff[NB1 + i] + 1024 * p;
          const unsigned lg = lbase + kh * 9216 + tg2 * 4608;
          if constexpr (p == 0) { gwo = gj(i) * (32 * 1152); asm volatile("" : "+s"(gwo)); }
          if constexpr (p < 4) bdmar<L, 0>(lg, vg[p], rd, gwo); else bdmar32<L, 0>(lg, vg[4], rd, gwo);
        }
        if constexpr (vk == V_GH) {
          constexpr int i = va >> 2, q = va & 3;
          if constexpr ((q & 1) == 0) { gho = gj(i) * 4096 + ((q >> 1) ^ kh) * 2048; asm volatile("" : "+s"(gho)); }
                    if constexpr (i < 16) asm volatile("buffer_load_dwordx4 %0, %1, %2, %3 offen offset:%4 sc1" : "=a"(hr[i][q]) : "v"(vh), "s"(rgh), "s"(gho), "i"((q & 1) * 1024));
          else asm volatile("buffer_load_dwordx4 %0, %1, %2, %3 offen offset:%4 sc1" : "=v"(hr[i][q]) : "v"(vh), "s"(rgh), "s"(gho), "i"((q & 1) * 1024));
        }
        if constexpr (vk == V_PL) asm volatile("buffer_load_dwordx2 %0, %1, %2, 0 offen sc1" : "=v"(pl[va]) : "v"(va == 4 ? vpl32 : vpl8), "s"(rf));
        if constexpr (vk == V_OW) oblock(IC<(va >> 4)>{}, IC<(va & 15)>{}, IC<((va >> 4) == 0 ? OB0 : OB1)>{}, lbase + wave * 9216);
                constexpr int ft = PG.ft[U][t], fo = PG.fo[U][t];
        if constexpr (ft == T_EA) {
          constexpr int grp = fo / 31, hp = grp >> 2, t2 = grp & 3, o = fo % 31;
          if constexpr (o < 30) eop(acc[4 * hp + 2 * (o / 15)][t2], acc[4 * hp + 2 * (o / 15) + 1][t2], o % 15, o / 15);
          else st16<hp * 4096, t2 * 1024>(vh, rhs, hw);
        }
        if constexpr (ft == T_SIG) { constexpr int q = fo / 12; eop(acc[8 + 2 * (q >> 3) + (q & 1)][(q >> 1) & 3], acch, fo % 12, 0); }
        if constexpr (ft == T_EB) {
          constexpr int grp = fo / 7, hp = grp >> 2, t2 = grp & 3, o = fo % 7;
          if constexpr (o < 6) eop(acc[8 + 2 * hp + o / 3][t2], acc[12 + 2 * hp + o / 3][t2], 12 + o % 3, o / 3);
          else st16<(2 + hp) * 4096, t2 * 1024>(vh, rhs, hw);
        }
        if constexpr (ft == T_EQ) {
          constexpr int tp = fo / 31, o = fo % 31;
          if constexpr (o < 30) eop(acc[4][2 * tp + o / 15], acc[5][2 * tp + o / 15], o % 15, o / 15);
          else st16s<tp * 2048>(vh, rh, (unsigned)(((4 * m + wave) * NJ + 18 * gam + 16 + (ci >> 1)) * 4096 + 1024 * (ci & 1)), hw);
        }
        if constexpr (ft == T_ATT) {
          if constexpr (fo < 4 * ATT_PREP) {
            constexpr int t2 = fo / ATT_PREP, o = fo % ATT_PREP;
            if constexpr (o == 0) { qf[t2][0] = pk2(acc[0][t2][0], acc[0][t2][1]); qf[t2][1] = pk2(acc[0][t2][2], acc[0][t2][3]); kf[t2][0] = pk2(acc[1][t2][0], acc[1][t2][1]); kf[t2][1] = pk2(acc[1][t2][2], acc[1][t2][3]); }
            if constexpr (o == 1) {
              PIN(acc[3][t2]);
              const unsigned L = pk2(acc[3][t2][2], acc[3][t2][3]);
              *(LAS unsigned*)(avw + t2 * 16 * AVS) = L;
              auto r = __builtin_amdgcn_permlane16_swap(L, L, false, false);
              qf[t2][2] = sel(mk[5], r[0], 0u); qf[t2][3] = 0u; kf[t2][2] = r[1]; kf[t2][3] = 0u; PIN(qf[t2]); PIN(kf[t2]);
            }
            if constexpr (o == 2) { *(LAS u32x2*)(av + t2 * 16 * AVS) = (u32x2){pk2(acc[2][t2][0], acc[2][t2][1]), pk2(acc[2][t2][2], acc[2][t2][3])}; }
          } else if constexpr (fo == 4 * ATT_PREP) {
            st16s<0>(vh, mksrd(ws + WS_Z + (4 * m + wave) * 256, c == 0 ? 256u : 0u), 0u,
                     (u32x4){__float_as_uint(acc[3][0][0]), __float_as_uint(acc[3][1][0]), __float_as_uint(acc[3][2][0]), __float_as_uint(acc[3][3][0])});
          } else if constexpr (fo == 4 * ATT_PREP + 1) {
            const int l2 = lane_id();
            if (wave < 3) *(LAS u32x4*)(lbase + AKX + (wave + 1) * 1024 + l2 * 16) = kf[3];
            const u32x2 hp = {pk2(acch[0], acch[1]), pk2(acch[2], acch[3])};
            auto r = __builtin_amdgcn_permlane16_swap(hp[1], hp[1], false, false);
            if (wave == 0) *(LAS u32x2*)(lbase + AKX + l2 * 16) = hp;
            if (wave == 1) *(LAS u32x2*)(av - 80 * AVS) = hp;
            if (wave == 2) { *(LAS u32x2*)(lbase + AKX + l2 * 16 + 8) = (u32x2){r[1], 0u}; if ((l2 >> 4) == 2) *(LAS unsigned*)(avw - 144 * AVS) = hp[1]; }
            asm volatile("s_waitcnt lgkmcnt(0)" ::: "memory");
          } else if constexpr (fo == 4 * ATT_PREP + 2) {
            kp = *(LAS u32x4*)(lbase + AKX + wave * 1024 + vh);
          } else if constexpr (fo == NATT - 1) {
            st4s<0>(vo2, rh, (unsigned)(((4 * m + wave) * NJ + 160) * 4096 + 256 * c), ol);
          } else {
            constexpr int q = fo - ATT0, t2 = q / ATT_TILE, o = q % ATT_TILE;
            static_assert(true, "");
            if constexpr (o == 0) {
              mma<'v', 'v', true>(s0, __builtin_bit_cast(bf16x8, t2 == 0 ? kp : kf[t2 == 0 ? 0 : t2 - 1]), __builtin_bit_cast(bf16x8, qf[t2]));
              mma<'v', 'v', true>(s1, __builtin_bit_cast(bf16x8, kf[t2]), __builtin_bit_cast(bf16x8, qf[t2]));
            }
            if constexpr (o == 1) { vf0 = cat(trhalf(avr, t2 * 16 * AVS), trhalf(avr, (t2 + 1) * 16 * AVS)); vf1 = cat(trhalf(avl, t2 * 16 * AVS), trhalf(avl, (t2 + 1) * 16 * AVS)); }
            if constexpr (o == 2) {
              asm volatile("s_nop 3" : "+v"(s0), "+v"(s1));
#pragma unroll
              for (int i = 0; i < 4; ++i) { const f32x4& sq = i < 2 ? s0 : s1; cv[i] = __uint_as_float(sel(mk[4], __float_as_uint(sq[2 * (i & 1) + 1]), __float_as_uint(sq[2 * (i & 1)]))); }
            }
            if constexpr (o == 3) {
              const bool first = t2 == 0 && m == 0 && wave == 0;
#pragma unroll
              for (int i = 0; i < 4; ++i) cv[i] = __uint_as_float(sel((first && i < 2) ? 0ull : mk[i], __float_as_uint(cv[i]), 0xff61b1e6u));
            }
            if constexpr (o == 4) { mx = red_max(fmaxf(fmaxf(cv[0], cv[1]), fmaxf(cv[2], cv[3]))); PIN(mx); }
            if constexpr (o == 5) {
              constexpr float SC = 0.23570226039551584f * LOG2E;
#pragma unroll
              for (int i = 0; i < 4; ++i) { cv[i] = (cv[i] - mx) * SC; PIN(cv[i]); }
            }
            if constexpr (o == 6) {
#pragma unroll
              for (int i = 0; i < 4; ++i) { cv[i] = __builtin_amdgcn_exp2f(cv[i]); PIN(cv[i]); }
            }
            if constexpr (o == 7) {
#pragma unroll
              for (int i = 0; i < 4; ++i) pw[i] = pk2(cv[i], cv[i]) & pmask;
              PIN(pw);
            }
            if constexpr (o == 8) {
              asm volatile("s_nop 0" : "+v"(pw)); mma<'v', 'v', true>(o0, vf0, __builtin_bit_cast(bf16x8, pw)); mma<'v', 'v', true>(o1, vf1, __builtin_bit_cast(bf16x8, pw));
            }
            if constexpr (o == 9) { sm = __builtin_amdgcn_rcpf(red_sum((cv[0] + cv[1]) + (cv[2] + cv[3]))); PIN(sm); }
            if constexpr (o == 10) {
              hw[2 * (t2 & 1)] = pk2(o0[0] * sm, o0[1] * sm); hw[2 * (t2 & 1) + 1] = pk2(o0[2] * sm, o0[3] * sm); PIN(hw);
              PIN(o1);
              const unsigned lv = pk2(o1[0] * sm, o1[1] * sm);
              ol = t2 == 0 ? lv : sel(0xffffull << (16 * t2), lv, ol); PIN(ol);
            }
            if constexpr (o == 11 && (t2 & 1)) st16s<(t2 >> 1) * 2048>(vh, rh, (unsigned)(((4 * m + wave) * NJ + 144 + (c >> 1)) * 4096 + 1024 * (c & 1)), hw);
          }
        }
        SB();
      });
    }
  });
  const int lane = lane_id();
  check(IC<PG.nu>{});
  u32x2 lf[4];
  {
    const unsigned so = (unsigned)(((2 * mt + (s >> 1)) * NJ + 144 + wave) * 4 + 2 * (s & 1)) * 1024;
    sfor<8>([&](auto Q) { constexpr int st = Q.v >> 1, q = Q.v & 1; ld16s<q * 1024>(of[st][q], vh, rh, so + st * 16384); });
    const unsigned sl = (unsigned)(((2 * mt + (s >> 1)) * NJ + 160 + (wave & 1)) * 4096 + 128 * (s & 1));
    const unsigned vl = (lane >> 4) * 1024 + (lane & 15) * 8;
    sfor<4>([&](auto Q) { ld8s<Q.v * 256>(lf[Q.v], vl, rh, sl); });
    asm volatile("buffer_load_dwordx2 %0, %1, %2, 0 offen sc1" : "=v"(zv) : "v"((lane & 15) * 16), "s"(mksrd(ws + WS_Z + (2 * mt + (s >> 1)) * 256 + 8 * (s & 1), 256)));
  }
  asm volatile("s_nop 7\n\ts_waitcnt lgkmcnt(0)" ::: "memory");
  BAR();
  SB();
  STAMP(5);
  const int grp = mt * 4 + nt, quarter = 2 * tg2 + kh;
  const unsigned xw = lbase + XOFF + wave * 18432, xr = lbase + XOFF + (wave ^ 2) * 18432;
#pragma unroll
  for (int j = 0; j < 9; ++j) { *(LAS f32x4*)(xw + lane * 16 + j * 2048) = ga[j][2]; *(LAS f32x4*)(xw + lane * 16 + j * 2048 + 1024) = ga[j][3]; }
  asm volatile("s_waitcnt lgkmcnt(0)" ::: "memory");
  BAR();
#pragma unroll
  for (int j = 0; j < 9; ++j) { ga[j][0] += *(LAS f32x4*)(xr + lane * 16 + j * 2048); ga[j][1] += *(LAS f32x4*)(xr + lane * 16 + j * 2048 + 1024); }
  const u32x4 rm = mksrd(ws + WS_MAIL + (size_t)grp * (16 * 9216), 16 * 9216);
  {
    const unsigned vo = lane * 16 + (quarter * 4 + s) * 9216;
    sfor<9>([&](auto J) { constexpr int j = J.v;
      st16<(j >> 2) * 4096, (j & 3) * 1024>(vo, rm, (u32x4){pk2(ga[j][0][0], ga[j][0][1]), pk2(ga[j][0][2], ga[j][0][3]), pk2(ga[j][1][0], ga[j][1][1]), pk2(ga[j][1][2], ga[j][1][3])}); });
  }
  asm volatile("s_waitcnt lgkmcnt(0)" ::: "memory");
  SB();
  STAMP(6);
  sfor<20>([&](auto Q) { oblock(IC<2 + Q.v / 10>{}, IC<Q.v % 10>{}, IC<(Q.v / 10) * 9216>{}, xr); });
  f32x4 oa[9][2];
  const unsigned trw = (4 * (lane >> 4) + ((lane & 15) >> 2)) * 288 + 8 * (lane & 3);
  auto ostep = [&](auto ST, unsigned base) {
    constexpr int st = decltype(ST)::v;
    bf16x8 wf[9];
#pragma unroll
    for (int j = 0; j < 9; ++j) wf[j] = cat(trhalf(base, 32 * j), trhalf(base, 32 * j + 4608));
#pragma unroll
    for (int j = 0; j < 9; ++j) { mma<'v', 'v', st == 0>(oa[j][0], wf[j], of[st][0]); mma<'v', 'v', st == 0>(oa[j][1], wf[j], of[st][1]); }
  };
  asm volatile("s_waitcnt vmcnt(29)" : "+v"(of[0][0]), "+v"(of[0][1]), "+v"(of[1][0]), "+v"(of[1][1]), "+v"(of[2][0]), "+v"(of[2][1]), "+v"(of[3][0]), "+v"(of[3][1]),
               "+v"(lf[0]), "+v"(lf[1]), "+v"(lf[2]), "+v"(lf[3]), "+v"(zv) :: "memory");
#pragma unroll
  for (int st = 0; st < 4; ++st) fix(of[st][0], of[st][1]);
#pragma unroll
  for (int t = 0; t < 2; ++t) { u32x4 v = {lf[0][t], lf[1][t], lf[2][t], lf[3][t]}; asm volatile("s_nop 1" : "+v"(v)); of[4][t] = __builtin_bit_cast(bf16x8, v); }
  ostep(IC<0>{}, lbase + OB0 + wave * 9216 + trw);
  ostep(IC<1>{}, lbase + OB1 + wave * 9216 + trw);
  asm volatile("s_waitcnt vmcnt(20) lgkmcnt(0)" ::: "memory");
  if (lane == 0)
    __hip_atomic_store((u64*)(ws + WS_FLAG + 4096 + grp * 128) + (quarter * 4 + s), ((u64)thi << 32) | (tlo + 1u), __ATOMIC_RELAXED, __HIP_MEMORY_SCOPE_AGENT);
  STAMP(7);
  if (wave < 2) {
    const unsigned o = (unsigned)(18 * 16 * wave) * 1152 + 288 * nt;
    const u32x4 rl = mksrd(wo + o, 576 * 1152 - o);
    sfor<9>([&](auto Q) { constexpr int p = Q.v;
      const int q = 64 * p + lane, rho = q / 18, e = 4 * (rho >> 4) + (rho & 3), gg = (rho >> 2) & 3;
      bdma<OB0 + 1024 * p, 0>(lbase + wave * 9216, (unsigned)((18 * (4 * gg + (e >> 1)) + 16 + (e & 1)) * 1152 + (q % 18) * 16), rl); });
    asm volatile("s_waitcnt vmcnt(9)" ::: "memory");
  } else asm volatile("s_waitcnt vmcnt(0)" ::: "memory");
  ostep(IC<2>{}, xr + trw);
  ostep(IC<3>{}, xr + 9216 + trw);
  if (wave < 2) { asm volatile("s_waitcnt vmcnt(0)" ::: "memory"); ostep(IC<4>{}, lbase + OB0 + wave * 9216 + trw); }
  asm volatile("s_nop 7\n\ts_waitcnt lgkmcnt(0)" ::: "memory");
  SB();
  float gt[2], om[2];
#pragma unroll
  for (int t = 0; t < 2; ++t) { gt[t] = __builtin_amdgcn_rcpf(1.f + __builtin_amdgcn_exp2f(zv[t] * -LOG2E)); om[t] = __builtin_amdgcn_rcpf(1.f + __builtin_amdgcn_exp2f(zv[t] * LOG2E)); }
  if (quarter == s) {
#pragma unroll
    for (int j = 0; j < 9; ++j)
#pragma unroll
      for (int t = 0; t < 2; ++t) *(LAS f32x4*)(xr + lane * 16 + j * 2048 + t * 1024) = oa[j][t] * gt[t] + ga[j][t] * om[t];
  } else {
#pragma unroll
    for (int j = 0; j < 9; ++j)
#pragma unroll
      for (int t = 0; t < 2; ++t) *(LAS f32x4*)(xr + lane * 16 + j * 2048 + t * 1024) = oa[j][t] * gt[t];
  }
  asm volatile("s_waitcnt lgkmcnt(0)" ::: "memory");
  BAR();
  STAMP(8);
  f32x4 fb[3][2];
#pragma unroll
  for (int i = 0; i < 3; ++i) {
    const int j = wave + 4 * i < 9 ? wave + 4 * i : 8;
#pragma unroll
    for (int t = 0; t < 2; ++t) {
      const unsigned aa = lbase + XOFF + lane * 16 + j * 2048 + t * 1024;
      fb[i][t] = (*(LAS f32x4*)(aa) + *(LAS f32x4*)(aa + 18432)) + (*(LAS f32x4*)(aa + 36864) + *(LAS f32x4*)(aa + 55296));
    }
  }
  {
    const u64* fp = (const u64*)(ws + WS_FLAG + 4096 + grp * 128) + s * 4 + (lane & 3);
    for (unsigned spin = 0; spin < (1u << 14); ++spin) {
      const u64 v = __hip_atomic_load(fp, __ATOMIC_RELAXED, __HIP_MEMORY_SCOPE_AGENT);
      if (__all((lane & 3) == s || v == (((u64)thi << 32) | (tlo + 1u)))) break;
      __builtin_amdgcn_s_sleep(1);
    }
    asm volatile("" ::: "memory");
  }
  STAMP(9);
  {
    const __amdgpu_buffer_rsrc_t mail = __builtin_amdgcn_make_buffer_rsrc(ws + WS_MAIL + (size_t)grp * (16 * 9216), 0, 16 * 9216, 0x00020000);
    u32x4 mv[3][3];
#pragma unroll
    for (int i = 0; i < 3; ++i) {
      const int j = wave + 4 * i < 9 ? wave + 4 * i : 8;
#pragma unroll
      for (int o = 0; o < 3; ++o) mv[i][o] = __builtin_amdgcn_raw_buffer_load_b128(mail, lane * 16, (s * 4 + o + (o >= s)) * 9216 + j * 1024, 16);
    }
#pragma unroll
    for (int i = 0; i < 3; ++i) {
      const int j = wave + 4 * i;
      if (j < 9) {
#pragma unroll
        for (int t = 0; t < 2; ++t) {
          float r[4];
#pragma unroll
          for (int e = 0; e < 4; ++e) {
            float sum = 0.f;
#pragma unroll
            for (int o = 0; o < 3; ++o) { const unsigned d = mv[i][o][2 * t + (e >> 1)]; sum += __uint_as_float((e & 1) ? (d & 0xffff0000u) : (d << 16)); }
            r[e] = fb[i][t][e] + om[t] * sum;
          }
          const int tok = 128 * mt + 32 * s + 16 * t + (lane & 15);
          *(u32x2*)(out + (size_t)tok * 1152 + (144 * nt + 16 * j + 4 * (lane >> 4)) * 2) = (u32x2){pk2(r[0], r[1]), pk2(r[2], r[3])};
        }
      }
    }
  }
  if (bid == 0 && wave == 3) {
    const u64* fp = (const u64*)(ws + WS_FLAG) + lane;
    for (unsigned spin = 0; spin < (1u << 14); ++spin) {
      bool okk = true;
#pragma unroll
      for (int i = 0; i < 4; ++i) { const u64 v = __hip_atomic_load(fp + 64 * i, __ATOMIC_RELAXED, __HIP_MEMORY_SCOPE_AGENT); okk &= (unsigned)(v >> 32) == thi && (unsigned)v - tlo - 1u <= 6u; }
      if (__all(okk)) break;
      __builtin_amdgcn_s_sleep(2);
    }
    if (lane == 0) __hip_atomic_store((u64*)(ws + WS_FLAG + 16384), ((u64)thi << 32) | tlo, __ATOMIC_RELAXED, __HIP_MEMORY_SCOPE_AGENT);
  }
  STAMP(10);
  asm volatile(".Lkf_code_end:");
}

extern "C" void kernel_launch(void* const* d_in, const int* in_sizes, int n_in, void* d_out, int out_size, void* d_ws, size_t ws_size, hipStream_t stream) {
  (void)hipFuncSetAttribute((const void*)kf, hipFuncAttributeMaxDynamicSharedMemorySize, LDS_BYTES);
  hipLaunchKernelGGL(kf, dim3(256), dim3(256), LDS_BYTES, stream, (const char*)d_in[0], (const char*)d_in[1], (const char*)d_in[2], (const char*)d_in[4],
                     (const char*)d_in[5], (const char*)d_in[6], (const char*)d_in[7], (char*)d_ws, (char*)d_out);
}
```

```cpp
#include <hip/hip_runtime.h>
#include <hip/hip_bf16.h>
#include <stdint.h>
#include <utility>
template <int N> struct IC { static constexpr int v = N; };
template <class F, int... Is> __device__ __forceinline__ void sfor_(F&& f, std::integer_sequence<int, Is...>) { (f(IC<Is>{}), ...); }
template <int N, class F> __device__ __forceinline__ void sfor(F&& f) { sfor_(f, std::make_integer_sequence<int, N>{}); }

typedef __bf16 bf16x2 __attribute__((ext_vector_type(2)));
typedef __bf16 bf16x4 __attribute__((ext_vector_type(4)));
typedef __bf16 bf16x8 __attribute__((ext_vector_type(8)));
typedef float f32x2 __attribute__((ext_vector_type(2)));
typedef float f32x4 __attribute__((ext_vector_type(4)));
typedef unsigned u32x2 __attribute__((ext_vector_type(2)));
typedef unsigned u32x4 __attribute__((ext_vector_type(4)));
typedef unsigned long long u64;
#define LAS __attribute__((address_space(3)))
#define DI __device__ __forceinline__
#define SB() __builtin_amdgcn_sched_barrier(0)
#define BAR() do { asm volatile("" ::: "memory"); __builtin_amdgcn_s_barrier(); asm volatile("" ::: "memory"); } while (0)
#define PIN(x) asm volatile("" : "+v"(x))

constexpr unsigned GS = 162 * 4096;
constexpr float LOG2E = 1.4426950408889634f;
constexpr float ZMAX = 40.f;
constexpr unsigned WS_HF = 0, HF_BYTES = 32u * GS;
constexpr unsigned WS_MAIL = 24u << 20;
constexpr unsigned WS_PK = 34u << 20;
constexpr unsigned WS_Z = 40u << 20;
constexpr unsigned WS_FLAG = 41u << 20;
constexpr int NLOC = 17, HXOFF = NLOC * 8192, CTL = HXOFF + 18432, PFOFF = CTL + 256, LDS_BYTES = 163840;
constexpr int AVS = 48, AKX = HXOFF + 272 * AVS;
static_assert(AKX + 5120 <= CTL, "attention staging");
constexpr int PARK = PFOFF + 256; static_assert(PARK + 5120 <= LDS_BYTES && PARK % 16 == 0, "park");
constexpr int LOFF = 7 * 18432, XOFF = 18432;

DI unsigned pk2(float lo, float hi) { f32x2 v = {lo, hi}; bf16x2 b = __builtin_convertvector(v, bf16x2); return __builtin_bit_cast(unsigned, b); }
DI bf16x4 trhalf(unsigned addr, int off) { return __builtin_amdgcn_ds_read_tr16_b64_v4bf16((LAS bf16x4*)(addr + off)); }
DI bf16x8 cat(bf16x4 a, bf16x4 b) { return __builtin_shufflevector(a, b, 0, 1, 2, 3, 4, 5, 6, 7); }
DI bf16x8 ld128(unsigned addr, int off) { return *(LAS bf16x8*)(addr + off); }
DI u32x4 mksrd(const void* p, unsigned bytes) { return (u32x4){(unsigned)(size_t)p, (unsigned)((size_t)p >> 32) & 0xffffu, bytes, 0x00020000u}; }
DI int lane_id() { int l = __builtin_amdgcn_mbcnt_hi(~0u, __builtin_amdgcn_mbcnt_lo(~0u, 0u)); asm volatile("" : "+v"(l)); return l; }
template <int L> DI void setm0(unsigned lw) { asm volatile("s_add_u32 m0, %0, %1" ::"s"(lw), "i"(L) : "scc"); }
template <int O> DI void bdma(unsigned voff, u32x4 srd, unsigned so) { asm volatile("buffer_load_dwordx4 %0, %1, %2 offen offset:%3 lds" ::"v"(voff), "s"(srd), "s"(so), "i"(O)); }
DI void bdmac(unsigned voff, u32x4 srd, unsigned so) { asm volatile("buffer_load_dwordx4 %0, %1, %2 offen sc1 lds" ::"v"(voff), "s"(srd), "s"(so)); }
DI void bdma32(unsigned voff, u32x4 srd, unsigned so) {
  asm volatile("s_mov_b32 exec_hi, 0\n\tbuffer_load_dwordx4 %0, %1, %2 offen lds\n\ts_mov_b32 exec_hi, -1" ::"v"(voff), "s"(srd), "s"(so));
}
DI void vdma(const void* vaddr) { asm volatile("global_load_lds_dwordx4 %0, off" ::"v"(vaddr)); }
DI void touch(unsigned voff, u32x4 srd, unsigned so) { asm volatile("buffer_load_dword %0, %1, %2 offen lds" ::"v"(voff), "s"(srd), "s"(so)); }
#define NOP0() asm volatile("s_nop 0")
template <int N> DI void waitv() { asm volatile("s_waitcnt vmcnt(%0)" ::"n"(N > 63 ? 63 : N) : "memory"); }
template <int R, int O> DI void ld16a(unsigned voff, u32x4 srd, unsigned so) {
  asm volatile("buffer_load_dwordx4 a[%c3:%c4], %0, %1, %2 offen offset:%c5 sc1" ::"v"(voff), "s"(srd), "s"(so), "i"(R), "i"(R + 3), "i"(O));
}
template <int O, class T> DI void ld16s(T& d, unsigned voff, u32x4 srd, unsigned so) {
  asm volatile("buffer_load_dwordx4 %0, %1, %2, %3 offen offset:%4 sc1" : "=v"(d) : "v"(voff), "s"(srd), "s"(so), "i"(O));
}
template <int O, class T> DI void ld8s(T& d, unsigned voff, u32x4 srd, unsigned so) {
  asm volatile("buffer_load_dwordx2 %0, %1, %2, %3 offen offset:%4 sc1" : "=v"(d) : "v"(voff), "s"(srd), "s"(so), "i"(O));
}
template <int O> DI void st16n(unsigned voff, u32x4 srd, unsigned so, u32x4 v) {
  asm volatile("buffer_store_dwordx4 %0, %1, %2, %3 offen offset:%4 sc1\n\ts_nop 1" ::"v"(v), "v"(voff), "s"(srd), "s"(so), "i"(O) : "memory");
}
DI void st8s(unsigned voff, u32x4 srd, u32x2 v) { asm volatile("buffer_store_dwordx2 %0, %1, %2, 0 offen sc1" ::"v"(v), "v"(voff), "s"(srd) : "memory"); }
DI void st4s(unsigned voff, u32x4 srd, unsigned so, unsigned v) { asm volatile("buffer_store_dword %0, %1, %2, %3 offen sc1" ::"v"(v), "v"(voff), "s"(srd), "s"(so) : "memory"); }
template <int N> DI void chkflag(u32x2& v, unsigned voff, u32x4 srd, u64 lo, u64 hi) {
  u64 t; unsigned cnt;
  asm volatile("s_waitcnt vmcnt(%7)\n\tv_cmp_gt_u64 vcc, %5, %0\n\tv_cmp_lt_u64 %1, %6, %0\n\ts_or_b64 vcc, vcc, %1\n\ts_cbranch_vccnz .Lkf_pslow%=\n.Lkf_pback%=:\n\t.subsection 1\n"
               ".Lkf_pslow%=:\n\ts_movk_i32 %2, 0x4000\n.Lkf_ploop%=:\n\tbuffer_load_dwordx2 %0, %3, %4, 0 offen sc1\n\ts_waitcnt vmcnt(0)\n\tv_cmp_gt_u64 vcc, %5, %0\n\tv_cmp_lt_u64 %1, %6, %0\n\t"
               "s_or_b64 vcc, vcc, %1\n\ts_cbranch_vccz .Lkf_pback%=\n\ts_sleep 1\n\ts_sub_u32 %2, %2, 1\n\ts_cbranch_scc0 .Lkf_ploop%=\n\ts_branch .Lkf_pback%=\n\t.subsection 0"
               : "+v"(v), "=&s"(t), "=&s"(cnt) : "v"(voff), "s"(srd), "s"(lo), "s"(hi), "n"(N) : "vcc", "scc", "memory");
}
DI unsigned vcvt(float a, float b) { unsigned r; asm volatile("v_cvt_pk_bf16_f32 %0, %1, %2" : "=v"(r) : "v"(a), "v"(b)); return r; }
DI unsigned vsel(u64 mask, unsigned a1, unsigned a0) { unsigned r; asm volatile("v_cndmask_b32 %0, %1, %2, %3" : "=v"(r) : "v"(a0), "v"(a1), "s"(mask)); return r; }
DI unsigned vsel0(u64 mask, unsigned a1) { unsigned r; asm volatile("v_cndmask_b32 %0, 0, %1, %2" : "=v"(r) : "v"(a1), "s"(mask)); return r; }
template <class T> DI T vmov(T a) { T r; asm volatile("v_mov_b32 %0, %1" : "=v"(r) : "v"(a)); return r; }
DI unsigned vzero() { unsigned r; asm volatile("v_mov_b32 %0, 0" : "=v"(r)); return r; }
template <class T> DI void vswap16(T& a, T& b) { asm volatile("s_nop 0\n\tv_permlane16_swap_b32 %0, %1" : "+v"(a), "+v"(b)); }
template <class T> DI void vswap32(T& a, T& b) { asm volatile("s_nop 0\n\tv_permlane32_swap_b32 %0, %1" : "+v"(a), "+v"(b)); }
DI float vmax(float a, float b) { float r; asm volatile("v_max_f32 %0, %1, %2" : "=v"(r) : "v"(a), "v"(b)); return r; }
DI float vmax3(float a, float b, float c) { float r; asm volatile("v_max3_f32 %0, %1, %2, %3" : "=v"(r) : "v"(a), "v"(b), "v"(c)); return r; }
DI float vmuls(float s, float a) { float r; asm volatile("v_mul_f32 %0, %1, %2" : "=v"(r) : "s"(s), "v"(a)); return r; }
DI float vmins(float s, float a) { float r; asm volatile("v_min_f32 %0, %1, %2" : "=v"(r) : "s"(s), "v"(a)); return r; }
template <int K, bool EQ> DI u64 smask(int w) { u64 r; asm("s_cmp_eq_u32 %1, %2\n\ts_cselect_b64 %0, %3, %4" : "=s"(r) : "s"(w), "i"(K), "i"(EQ ? -1 : 0), "i"(EQ ? 0 : -1) : "scc"); return r; }
DI unsigned vadds(unsigned s, unsigned a) { unsigned r; asm volatile("v_add_u32 %0, %1, %2" : "=v"(r) : "s"(s), "v"(a)); return r; }
DI float vmul(float a, float b) { float r; asm volatile("v_mul_f32 %0, %1, %2" : "=v"(r) : "v"(a), "v"(b)); return r; }
DI float vfmas(float a, float s, float b) { float r; asm volatile("v_fma_f32 %0, %1, %2, %3" : "=v"(r) : "v"(a), "s"(s), "v"(b)); return r; }
DI float vadd1(float a) { float r; asm volatile("v_add_f32 %0, 1.0, %1" : "=v"(r) : "v"(a)); return r; }
DI float vexp(float a) { float r; asm volatile("v_exp_f32 %0, %1" : "=v"(r) : "v"(a)); return r; }
DI float vrcp(float a) { float r; asm volatile("v_rcp_f32 %0, %1" : "=v"(r) : "v"(a)); return r; }
DI unsigned vand(unsigned a, unsigned b) { unsigned r; asm volatile("v_and_b32 %0, %1, %2" : "=v"(r) : "v"(a), "v"(b)); return r; }
DI void mmac(f32x4& d, u32x4 a, u32x4 b, f32x4 c) { asm volatile("v_mfma_f32_16x16x32_bf16 %0, %1, %2, %3" : "=&v"(d) : "v"(a), "v"(b), "v"(c)); }
template <bool Z> DI void mma(f32x4& acc, bf16x8 a, bf16x8 b) {
  if constexpr (!Z) asm volatile("v_mfma_f32_16x16x32_bf16 %0, %1, %2, %0" : "+v"(acc) : "v"(a), "v"(b));
  else asm volatile("v_mfma_f32_16x16x32_bf16 %0, %1, %2, 0" : "=v"(acc) : "v"(a), "v"(b));
}
template <int B, bool Z> DI void mma_va(f32x4& acc, bf16x8 a) {
  if constexpr (!Z) asm volatile("v_mfma_f32_16x16x32_bf16 %0, %1, a[%c2:%c3], %0" : "+v"(acc) : "v"(a), "i"(B), "i"(B + 3));
  else asm volatile("v_mfma_f32_16x16x32_bf16 %0, %1, a[%c2:%c3], 0" : "=v"(acc) : "v"(a), "i"(B), "i"(B + 3));
}
template <int C, int B, bool Z> DI void mma_aa(bf16x8 a) {
  if constexpr (!Z) asm volatile("v_mfma_f32_16x16x32_bf16 a[%c1:%c2], %0, a[%c3:%c4], a[%c1:%c2]" ::"v"(a), "i"(C), "i"(C + 3), "i"(B), "i"(B + 3));
  else asm volatile("v_mfma_f32_16x16x32_bf16 a[%c1:%c2], %0, a[%c3:%c4], 0" ::"v"(a), "i"(C), "i"(C + 3), "i"(B), "i"(B + 3));
}
template <int C> DI void mma_av(bf16x8 a, bf16x8 b) { asm volatile("v_mfma_f32_16x16x32_bf16 a[%c2:%c3], %0, %1, a[%c2:%c3]" ::"v"(a), "v"(b), "i"(C), "i"(C + 3)); }
DI void mma16(f32x4& acc, bf16x4 a, u32x2 b) { asm volatile("v_mfma_f32_16x16x16_bf16 %0, %1, %2, %0" : "+v"(acc) : "v"(a), "v"(b)); }
template <int O> DI void stout(char* p, u32x2 v) { asm volatile("global_store_dwordx2 %0, %1, off offset:%2" ::"v"(p), "v"(v), "i"(O) : "memory"); }
template <int R, int O> DI void lds_to_a(unsigned addr) { asm volatile("ds_read_b128 a[%c1:%c2], %0 offset:%c3" ::"v"(addr), "i"(R), "i"(R + 3), "i"(O) : "memory"); }
template <int R, int O> DI void a_to_lds(unsigned addr) { asm volatile("ds_write_b128 %0, a[%c1:%c2] offset:%c3" ::"v"(addr), "i"(R), "i"(R + 3), "i"(O) : "memory"); }
constexpr int AX(int ks, int t) { return 16 * ks + 4 * t; }
constexpr int AH(int i, int t) { return 16 * (i < 11 ? i : i - 11) + 4 * t; }
constexpr int AG(int j, int t) { return 176 + 8 * j + 4 * (t - 2); }
DI void fix(bf16x8& a, bf16x8& b) {
  u32x4 x = __builtin_bit_cast(u32x4, a), y = __builtin_bit_cast(u32x4, b);
  u32x2 lo, hi;
  asm volatile("v_mov_b64 %0, %1" : "=v"(lo) : "v"(__builtin_shufflevector(y, y, 0, 1)));
  asm volatile("v_mov_b64 %0, %1" : "=v"(hi) : "v"(__builtin_shufflevector(x, x, 2, 3)));
  asm volatile("" : "+v"(x), "+v"(y));
  a = __builtin_bit_cast(bf16x8, __builtin_shufflevector(__builtin_shufflevector(x, x, 0, 1), lo, 0, 1, 2, 3));
  b = __builtin_bit_cast(bf16x8, __builtin_shufflevector(hi, __builtin_shufflevector(y, y, 2, 3), 0, 1, 2, 3));
  asm volatile("" : "+v"(a), "+v"(b));
}
DI int kap(int rho) { return 8 * ((rho >> 2) & 3) + 4 * (rho >> 4) + (rho & 3); }

constexpr int A0RATE = 20, ARATE = 14, RATE = 13, GRATE = 15, ACAP = 27, GCAP = 64, MISSX = 3;
constexpr int ATL = 10;
constexpr int PUBLAG = 14, PLEAD = 14;
constexpr int KSST = 4, PFST = 11, PFSP = 8, HST = 16, H8ST = 21, OST = 15;
constexpr int RFO = 32768, RFS = 128;
constexpr int PD = 4;
constexpr int NS1 = 24, NG = 20, NST = NS1 + NG, NB1 = 54;
constexpr int NLEV = 6;
enum { K_NORM = 0, K_SEL = 1, K_HALO = 2, K_G = 3 };
enum { M_NONE = 0, V_WB, V_XP, V_HX, V_GW, V_OW, V_PF, V_LASTDMA = V_PF, V_GH, V_PL, V_FL, V_KL, V_KS, V_LB, V_ST, V_LAST = V_ST,
       S_ARR, S_PV, S_CMP, S_BR, S_KW, S_XW, P_SO, F_XR, F_XH, F_SEL, F_FIX, F_E, F_ATT, F_CHK };
enum { T_EA = 0, T_SIG, T_EB, T_EQ };
struct Unit { int stage, blk, ks, base, slot, kind, koff; };
struct MO { int k, a; };
constexpr int MAXU = 620, MAXM = 4;
struct Prog {
  int nu; Unit u[MAXU];
  int sfirst[NST + 1];
  int bstage[NB1 + NG], blast[NB1 + NG], boff[NB1 + NG], biss[NB1 + NG];
  MO m[MAXU][4][MAXM]; int nm[MAXU][4]; int used[MAXU][4];
  int nwait[NST], nfirst, chkw[NLEV + 1], kwait, xwait[36];
  int over;
  int err;
};
constexpr int w0(const Prog& P, int n) { const int U = P.sfirst[n + 1] - P.sfirst[n]; return P.sfirst[n] + (U == 15 ? 6 : (n < 9 ? 12 : U / 2)); }
constexpr int arr(const Prog& P, int n) { int a = n >= NS1 ? P.sfirst[n] + 1 : w0(P, n) - 4; while (P.u[a].kind != K_NORM && P.u[a].kind != K_G) --a; if (P.u[a + 1].kind == K_SEL) --a; return a; }
constexpr int xru(int i) { constexpr int u[8] = {2, 3, 4, 5, 6, 7, 9, 11}; return u[i]; }
constexpr int ngap(const Prog& P, int U) { return P.u[U].kind == K_HALO ? 1 : 4; }
constexpr int cap(const Prog& P, int U, int t, int lv4) {
  const int lvl = (lv4 + (U & 3)) >> 2;
  int c = P.u[U].kind == K_HALO ? 1 : (t < 2 ? (P.u[U].kind == K_SEL ? 1 : 2) : (t == 2 ? 1 : 0));
  if (lvl >= 1 && t == 2) c = 2;
  if (lvl >= 2 && t == 3) c = 1;
  if (lvl >= 3) c = t == 3 ? 2 : 3;
  return c;
}
constexpr int ecost(int o) { return ((o >> 2) == 1 || (o >> 2) == 3) ? 2 : 1; }
constexpr int AR1 = 10, AR2 = 15, AR3 = 62;
constexpr int att_cost(int r) {
  if (r == 8 || r == 23 || r == 26) return 2;
  if (r == 14 || r == 15 || r == 49 || r == 50) return 2;
  if ((r >= 33 && r < 37) || r == 51 || r == 64) return 2;
  return 1;
}
constexpr Prog make_prog() {
  Prog P{};
  int nu = 0;
  auto add = [&](int st, int blk, int ks, int base, int slot, int kind, int koff) { P.u[nu] = Unit{st, blk, ks, base, slot, kind, koff}; ++nu; };
  for (int n = 0; n < 9; ++n) {
    P.sfirst[n] = nu;
    for (int u = 0; u < 16; ++u) { const int hp = u >> 3, pb = (u >> 2) & 1, isU = (u >> 1) & 1, kk = u & 1; add(n, 2 * n + isU, 2 * n + kk, isU ? 12 + 2 * hp + pb : 2 * hp + pb, 4 * hp + 2 * pb + isU, K_NORM, kk * 4096); }
  }
  for (int n = 9; n < 15; ++n) {
    P.sfirst[n] = nu;
    const int e = (n - 9) % 3, fb = 18 + 3 * (n - 9), s0 = n < 12 ? 8 : 12;
    const bool skew = n == 9 || n == 14 || n == 12;
    for (int u = 0; u < 24; ++u) {
      const int ks = (skew ? 2 * ((u % 12) >> 2) : 2 * (u >> 3)) + (u & 1), tile = skew ? 2 * (u / 12) + ((u >> 1) & 1) : (u >> 1) & 3;
      add(n, fb + (ks >> 1), 6 * e + ks, tile, s0 + tile, K_NORM, (ks & 1) * 4096);
    }
  }
  for (int n = 15; n < NS1; ++n) {
    P.sfirst[n] = nu;
    if (n == 21) { for (int ks = 12; ks < 18; ++ks) { add(n, 36 + ks, ks, 8, 4, K_NORM, 0); add(n, 36 + ks, ks, 9, 5, K_NORM, 0); } continue; }
    const int k0 = n == 15 ? 0 : (n == 16 ? 3 : (n < 21 ? 2 * (n - 15) : 12 + 3 * (n - 22))), nk = n == 16 ? 1 : ((n == 15 || n > 21) ? 3 : 2);
    for (int ks = k0; ks < k0 + nk; ++ks) {
      const int b = 36 + ks;
      add(n, b, ks, 4, 0, K_NORM, 0); add(n, b, ks, 5, 1, K_NORM, 0); add(n, b, ks, 6, 2, K_NORM, 0);
      if (n > 16 && n < 21) { add(n, b, ks, 8, 4, K_NORM, 0); add(n, b, ks, 9, 5, K_NORM, 0); }
      add(n, b, ks, 7, 3, K_SEL, 0); add(n, b, ks, 10, 6, K_HALO, 0);
    }
    if (n == 16) for (int ks = 0; ks < 4; ++ks) { add(n, 36 + ks, ks, 8, 4, K_NORM, 0); add(n, 36 + ks, ks, 9, 5, K_NORM, 0); }
  }
  for (int i = 0; i < NG; ++i) {
    const int n = NS1 + i; P.sfirst[n] = nu;
    for (int j = 0; j < 9; ++j) add(n, NB1 + i, i, 11, j, K_G, 32 * j);
  }
  P.sfirst[NST] = nu; P.nu = nu;
  for (int U = nu - 1; U >= 0; --U) P.bstage[P.u[U].blk] = P.u[U].stage;
  for (int U = 0; U < nu; ++U) P.blast[P.u[U].blk] = P.u[U].stage;
  for (int U = 0; U < nu; ++U) {
    if (P.u[U].kind == K_HALO) continue;
    for (int h = 0; h < (P.u[U].kind == K_SEL ? 2 : 1); ++h) {
      const int T = U + PD + h; if (T >= nu) continue;
      const int fs = P.bstage[P.u[T].blk], n = P.u[U].stage;
      if (fs > n + 1) P.err |= 65536;
      if (fs == n + 1 && (U < w0(P, n) || (U == w0(P, n) && P.u[U].kind == K_SEL))) P.err |= 65536;
    }
  }
  int freeat[NLOC] = {};
  for (int l = 0; l < NLOC; ++l) freeat[l] = l < 9 ? -1 : 8;
  for (int b = 0; b < NB1; ++b) {
    const int l = b <= 24 ? (b + 2) % 9 : (b - 16) % 17;
    P.boff[b] = l * 8192; P.biss[b] = freeat[l]; freeat[l] = P.blast[b] + 1;
  }
  for (int i = 0; i < NG; ++i) {
    const int b = NB1 + i, off = ((i + 2) % 7) * 18432; int fr = 0;
    if (i < 7) { for (int l = off / 8192; l <= (off + 18431) / 8192; ++l) if (freeat[l] > fr) fr = freeat[l]; }
    else fr = P.bstage[b - 7] + 1;
    P.boff[b] = off; P.biss[b] = fr;
  }
  auto put = [&](int U, int t, int k, int a, int c) {
    if (P.nm[U][t] >= MAXM) { P.err |= 1; return; }
    P.m[U][t][P.nm[U][t]++] = MO{k, a}; P.used[U][t] += c;
  };
  int hu[NG] = {};
  for (int i = 0; i < NG; ++i) {
    int dead = 0;
    for (int U = 0; U < nu; ++U) if (i < 11 ? (P.u[U].kind != K_G && P.u[U].ks == i) : (P.u[U].kind == K_G && P.u[U].ks == i - 11)) dead = U;
    hu[i] = dead + 1;
    if (hu[i] < P.sfirst[HST]) hu[i] = P.sfirst[HST];
    if (i >= 8 && hu[i] < P.sfirst[H8ST]) hu[i] = P.sfirst[H8ST];
    if (i >= 12 && hu[i] < P.sfirst[NS1 + 4]) hu[i] = P.sfirst[NS1 + 4];
    if (i >= 16) hu[i] = P.sfirst[NS1 + 10];
    if (i >= 18) hu[i] = P.sfirst[NS1 + OST] + 5;
    ++hu[i];
  }
  for (int n = 0; n + 1 < NST; ++n) {
    const int f = P.sfirst[n], W = w0(P, n);
    put(arr(P, n), 0, S_ARR, n, 2);
    put(W - (P.u[W - 2].kind == K_HALO ? 3 : 2), 0, S_PV, n, 1);
    put(W, 0, S_CMP, n, 2); put(W, 1, S_BR, n, 1);
    if (n < 8) for (int i = 0; i < 8; ++i) { if (!(i & 1)) put(f + xru(i), 0, S_XW, 4 * (n + 1) + (i >> 1), 1); put(f + xru(i), 0, F_XR, 8 * n + i, 1); }
  }
  for (int U = 0; U < nu; ++U) {
    if (P.u[U].kind == K_SEL) { put(U - 1, 0, F_SEL, 2 * U, 2); put(U - 1, 1, F_SEL, 2 * U + 1, 2); put(U - 3, 1, F_XH, 32 + P.u[U].ks, 1);
      int V = U - 4; if (P.u[U - 6].kind == K_HALO) V = U - 6;
      put(V, 0, F_XH, P.u[U].ks, 1); }
    if (P.u[U].kind == K_G && P.u[U].ks >= 15 && P.u[U].ks < NG - 1 && P.u[U].slot == 6) { put(U, 0, F_FIX, 2 * P.u[U].ks + 2, 3); put(U, 1, F_FIX, 2 * P.u[U].ks + 3, 3); }
  }
  struct Op { int e, kind, arg, prep, dl, cost; };
  Op sw[400] = {}, sx[240] = {}; int nw = 0, nx = 0;
  for (int b = 2; b < NB1; ++b) for (int h = 0; h < 2; ++h) {
    int e0 = P.biss[b] < 0 ? 1 : w0(P, P.biss[b]) + 1; if (b >= 36 && e0 < P.sfirst[11] + 3) e0 = P.sfirst[11] + 3;
    const bool miss = b >= 18 && (b < 36 ? (b - 18) % 9 < 3 : b - 36 < 5);
    sw[nw++] = Op{e0, V_WB, 2 * b + h, (h == 0 && (b >= 18 || !(b & 1))) ? 1 : 0, arr(P, P.bstage[b] - 1), (miss && b > 20) ? (b < 36 ? 16 * MISSX : 32) : 16};
  }
  for (int i = 0; i < NG; ++i) for (int p = 0; p < 5; ++p) sw[nw++] = Op{w0(P, P.biss[NB1 + i]) + 1, V_GW, 8 * i + p, p == 0, arr(P, NS1 + i - 1), p < 4 ? 21 : 11};
  for (int p = 0; p < 2; ++p) sw[nw++] = Op{w0(P, NS1) + 1, V_OW, p, 0, arr(P, NST - 2), p ? 11 : 21};
  for (int d = 2; d < 9; ++d) {
    for (int i = 0; i < 8; ++i) sx[nx++] = Op{d == 2 ? 1 : P.sfirst[d - 3] + xru(i | 1) + PD, V_XP, 8 * d + i, 1, P.sfirst[d - 1] + xru(i & 6), 16};
    if (d == 2) for (int i = 0; i < 5; ++i) sx[nx++] = Op{1, V_KL, i, 1, P.sfirst[1] + 14 + 3 * i, 16};
    if (d - 2 == KSST) for (int i = 0; i < 5; ++i) sx[nx++] = Op{P.sfirst[KSST] + 1, V_KS, i, 1, P.sfirst[KSST] + 8, 4};
  }
  put(P.sfirst[KSST], 0, S_KW, 0, 1);
  for (int i = 0; i < 5; ++i) sx[nx++] = Op{P.sfirst[9] + 12, V_HX, i, 1, P.sfirst[12], 16};
  for (int U = P.sfirst[9]; U < nu; ++U) {
    for (int i = 0; i < 7; ++i) if (U == P.sfirst[PFST] + 1 + PFSP * i) sx[nx++] = Op{U, V_PF, i, 1, U + 40, 48};
    for (int i = 0; i < NG; ++i) if (hu[i] == U) for (int q = 0; q < 4; ++q) sx[nx++] = Op{U, V_GH, 4 * i + q, q == 0, arr(P, NS1 + i - 1), (i < 2 || i == 8 || i == 12) ? 8 * MISSX + 8 : 16};
    if (hu[19] == U) for (int q = 0; q < 3; ++q) sx[nx++] = Op{U, V_LB, q, 0, nu - 4, 8};
  }
  int endK = 0, fu[NLEV + 1] = {};
  {
    int iw = 0, ix = 0, tok = 0; bool lastw = false;
    for (int U = 1; U < nu; ++U) {
      const int n = P.u[U].stage;
      tok += P.u[U].kind == K_HALO ? RATE / 4 : (n < 1 ? A0RATE : (n < 8 ? ARATE : (n < NS1 ? RATE : GRATE))); { const int cp = n < 1 ? 64 : (n < 6 ? ACAP : (n < 15 ? 48 : GCAP)); if (tok > cp) tok = cp; }
      if (P.u[U].kind == K_HALO || P.u[U].kind == K_SEL) continue;
      int placed = 0;
      for (int t = 0; t < 2; ++t) {
        if (P.used[U][t] != 0) continue;
        const bool cw = iw < nw && sw[iw].e <= U, cx = ix < nx && sx[ix].e <= U;
        if (!cw && !cx) break;
        bool usew = cw && (!cx || sw[iw].dl <= sx[ix].dl);
        if (placed && usew == lastw && (usew ? sw[iw].prep : sx[ix].prep)) {
          if (!(usew ? cx : cw)) continue;
          usew = !usew;
        }
        const Op o = usew ? sw[iw] : sx[ix];
        if (t == 0 && P.used[U][1] == 0 && ((n >= 8 && n < 15) || (tok < o.cost + 16 && o.dl > U + 8))) continue;
        if (tok < o.cost && (o.dl > U + 14 || (placed && o.dl > U + 8))) break;
        if (usew) ++iw; else ++ix;
        lastw = usew; tok -= o.cost;
        if (o.prep) { int Up = U - 1; if (P.u[Up].kind == K_HALO) put(Up, 0, P_SO, o.kind * 1024 + o.arg, 1); else put(Up, 2, P_SO, o.kind * 1024 + o.arg, o.kind == V_KL || o.kind == V_HX ? 1 : 0); }
        put(U, t, o.kind, o.arg, 2); ++placed;
        if (o.kind == V_KS) endK = U;
        { const int lv = (o.kind == V_WB && o.arg == 72) ? 1 : (o.kind != V_GH ? 0 : (o.arg == 0 ? 2 : (o.arg == 32 ? 3 : (o.arg == 48 ? 4 : (o.arg == 64 ? 5 : (o.arg == 72 ? 6 : 0)))))); if (lv) fu[lv] = U; }
      }
    }
    if (iw < nw || ix < nx) P.err |= 2;
  }
  for (int p = 1; p <= NLEV; ++p) {
    put(fu[p] - (P.u[fu[p] - 1].kind == K_HALO ? 2 : 1), 2, F_CHK, p, 3);
    int V = fu[p] - PLEAD, t = 0;
    while ((P.u[V].kind != K_NORM && P.u[V].kind != K_G) || P.used[V][t] != 0) { if (++t > 1) { t = 0; --V; } }
    put(V, t, V_PL, p, 2);
  }
  int pend[32] = {}, ph = 0, pt = 0, stU[4][8] = {};
  auto etask = [&](int id, int g0, int g1, int o0, int o1, bool store, int U0, int Uend, int errbit) -> int {
    for (int lvl = 0; lvl <= 12; ++lvl) {
      for (int pass = 0; pass < 2; ++pass) {
        int g[2] = {g0, g0}, o[2] = {o0, o0}, done = g0, U = U0, ph2 = ph, pt2 = pt;
        for (; U < nu; ++U) {
          for (int t = 0; t < ngap(P, U); ++t) {
            int room = cap(P, U, t, lvl) - P.used[U][t]; int cnt = P.nm[U][t];
            if (ph2 < pt2 && room >= 2 && cnt < MAXM) {
              if (pass) { put(U, t, V_ST, pend[ph2], 2); stU[pend[ph2] >> 6][pend[ph2] & 7] = U; }
              ++ph2; continue;
            }
            for (;;) {
              if (room <= 0 || cnt >= MAXM) break;
              int pick = -1;
              for (int pref = 0; pref < 2 && pick < 0; ++pref)
                for (int q = 0; q < 2; ++q) {
                  const int sI = (g[0] * 32 + o[0] <= g[1] * 32 + o[1] + 4) ? q : 1 - q;
                  if (g[sI] >= g1) continue;
                  const int cst = ecost(o[sI]);
                  if (cst > room) continue;
                  if (pref == 0 && room >= 2 && cst == 1) continue;
                  pick = sI; break;
                }
              if (pick < 0) break;
              if (pass) put(U, t, F_E, ((id * 16 + g[pick]) * 2 + pick) * 32 + o[pick], ecost(o[pick]));
              room -= ecost(o[pick]); ++cnt;
              if (++o[pick] == o1) { o[pick] = o0; ++g[pick]; }
            }
            while (store && done < g[0] && done < g[1]) { pend[pt2++] = id * 64 + done; ++done; }
          }
          if (g[0] >= g1 && g[1] >= g1 && (g0 < g1 || ph2 == pt2)) break;
        }
        if (pass == 0 && U >= Uend && lvl < 12) break;
        if (pass == 1) { if (U >= Uend) P.err |= errbit; ph = ph2; pt = pt2; return U; }
      }
    }
    return nu;
  };
  int e = 0;
  e = etask(T_EA, 0, 2, 0, 22, true, P.sfirst[8] + 9, P.sfirst[9] + 12, 4);
  e = etask(T_EA, 2, 8, 0, 22, true, e + 1, P.sfirst[13] - 14, 8);
  e = etask(T_SIG, 0, 4, 0, 16, false, e + 1 > P.sfirst[12] + 1 ? e + 1 : P.sfirst[12] + 1, P.sfirst[14] + 13, 32);
  e = etask(T_EB, 0, 4, 16, 22, true, e + 1 > P.sfirst[14] + 13 ? e + 1 : P.sfirst[14] + 13, P.sfirst[15], 128);
  e = etask(T_EB, 4, 8, 0, 22, true, (e > P.sfirst[15] ? e : P.sfirst[15]) + 1, P.sfirst[21] + 8, 64);
  e = etask(T_EB, 0, 0, 0, 0, false, e + 1, P.sfirst[22] + 8, 256);
  e = etask(T_EQ, 0, 2, 0, 22, true, e + 1 > P.sfirst[22] + 1 ? e + 1 : P.sfirst[22] + 1, P.sfirst[NS1] + 6, 512);
  const int eq = e;
  etask(T_EQ, 0, 0, 0, 0, false, e + 1, nu, 256);
  e = eq;
  const int endA = stU[T_EA][7], endB0 = stU[T_EB][3], endB = stU[T_EB][7], endQ = stU[T_EQ][1];
  int at = 0;
  auto aput = [&](int& U, int k, int a, int cst) {
    while (cap(P, U, at, ATL) - P.used[U][at] < cst || P.nm[U][at] >= MAXM) { if (++at >= ngap(P, U)) { at = 0; ++U; } }
    put(U, at, k, a, cst);
  };
  auto arows = [&](int& U, int r0, int r1) {
    for (int r = r0; r < r1; ++r) {
      for (int q = 0; q < 4; ++q) aput(U, F_ATT, 4 * r + (r == 15 ? (q + 1) & 3 : q), att_cost(r));
      if (r == 15 || r == 50) { U += 1; at = 0; }
    }
  };
  e = (e > P.sfirst[NS1] ? e : P.sfirst[NS1]) + 1;
  aput(e, F_ATT, 1099, 1);
  for (int o = 0; o < 5; ++o) { aput(e, F_ATT, 1100 + o, 1); aput(e, F_ATT, o < 4 ? o : 1110, 1); }
  arows(e, 1, 3); aput(e, F_ATT, 1111, 1);
  for (int o = 5; o < 10; ++o) aput(e, F_ATT, 1100 + o, 1);
  arows(e, 3, 5);
  for (int o = 12; o < 21; ++o) aput(e, F_ATT, 1100 + o, o == 15 || o == 17 ? 2 : 1);
  if (e <= w0(P, NS1)) { e = w0(P, NS1) + 1; at = 0; }
  arows(e, 5, AR1);
  for (int o = 21; o < 24; ++o) aput(e, F_ATT, 1100 + o, 1);
  const int sa = P.u[e].stage + (e < arr(P, P.u[e].stage) ? 0 : 1);
  aput(e, V_ST, 9 * 64, 2);
  arows(e, AR1, AR2);
  arows(e, 62, 65);
  if (e <= w0(P, sa)) { e = w0(P, sa) + 1; at = 0; }
  aput(e, F_ATT, 1001, 1);
  arows(e, AR2, 52);
  arows(e, 65, 66);
  arows(e, 52, AR3);
  ++e; at = 0;
  aput(e, V_ST, 8 * 64 + 0, 2); aput(e, V_ST, 8 * 64 + 2, 2); e += 5; at = 0; aput(e, V_ST, 8 * 64 + 1, 2);
  const int endO = e;
  const int ends[NLEV + 1] = {0, endK, endA, endB0, endB, endQ, endO}; int pubst[NLEV + 1] = {};
  for (int lev = 1; lev <= NLEV; ++lev) {
    pubst[lev] = P.u[ends[lev]].stage; while (arr(P, pubst[lev]) < ends[lev] + PUBLAG) ++pubst[lev];
    if (pubst[lev] >= NST - 1) { P.err |= 8192; pubst[lev] = NST - 2; }
    int U = w0(P, pubst[lev]) + 1, t = 0;
    while (P.used[U][t] != 0 || P.u[U].kind == K_HALO || P.u[U - 1].kind == K_HALO) { if (++t > 1) { t = 0; ++U; } if (U > w0(P, pubst[lev]) + 4) { U = w0(P, pubst[lev]) + 1; t = 2; break; } }
    put(U - 1, 2, P_SO, V_FL * 1024 + lev, 1); put(U, t, V_FL, lev, 2);
  }
  int pos = 1, poskl = 0, posow = 0; int posW[NB1 + NG] = {}, posX[36] = {}, posXW[36] = {}, posH[NG] = {}, posPL[NLEV + 1] = {}, poshx = -1, posArr[NST] = {}, posChk[NLEV + 1] = {}, posEnd[NLEV + 1] = {};
  pos += 12; const int px0 = pos - 1;
  for (int i = 0; i < 8; ++i) posX[4 + i / 2] = pos++;
  P.nfirst = pos - 1 - px0;
  for (int U = 0; U < nu; ++U) {
    for (int t = 0; t < 4; ++t) {
      if (t < ngap(P, U) && P.used[U][t] > cap(P, U, t, 0)) P.over += P.used[U][t] - cap(P, U, t, 0);
      for (int i = 0; i < P.nm[U][t]; ++i) {
        const int k = P.m[U][t][i].k, a = P.m[U][t][i].a;
        if (k == S_ARR) posArr[a] = pos;
        if (k == S_XW) posXW[a] = pos;
        if (k == S_KW) P.kwait = pos - 1 - poskl;
        if (k == V_KL) poskl = pos;
        if (k == F_CHK) posChk[a] = pos;
        if (k > V_LAST) continue;
        if (k == V_WB) posW[a >> 1] = pos;
        if (k == V_XP) posX[a >> 1] = pos;
        if (k == V_HX) poshx = pos;
        if (k == V_GW) posW[NB1 + (a >> 3)] = pos;
        if (k == V_GH) posH[a >> 2] = pos;
        if (k == V_OW) posow = pos;
        if (k == V_PL) posPL[a] = pos;
        ++pos;
      }
    }
    for (int lev = 1; lev <= NLEV; ++lev) if (ends[lev] == U) posEnd[lev] = pos - 1;
  }
  for (int n = 0; n + 1 < NST; ++n) {
    int last = -1;
    for (int b = 0; b < NB1 + NG; ++b) if (P.bstage[b] == n + 1 && posW[b] > last) last = posW[b];
    if (n + 1 == 15 && poshx > last) last = poshx;
    if (n == NST - 2 && posow > last) last = posow;
    if (n + 1 >= NS1 && posH[n + 1 - NS1] > last) last = posH[n + 1 - NS1];
    for (int lev = 1; lev <= NLEV; ++lev) if (pubst[lev] == n && posEnd[lev] > last) last = posEnd[lev];
    P.nwait[n] = posArr[n] - 1 - last;
    if (P.nwait[n] < 0) P.err |= 2048;
  }
  for (int i = 4; i < 36; ++i) { P.xwait[i] = posXW[i] - 1 - posX[i]; if (P.xwait[i] < 0) P.err |= 16384; }
  for (int p = 1; p <= NLEV; ++p) { P.chkw[p] = posChk[p] - 1 - posPL[p]; if (P.chkw[p] < 0) P.err |= 4096; }
  return P;
}
constexpr Prog PG = make_prog();
static_assert(PG.err == 0, "schedule");
constexpr int rd_abs(int U) { return PG.boff[PG.u[U].blk] + PG.u[U].koff; }
constexpr int rd_win(int U) { return PG.u[U].kind == K_G ? (rd_abs(U) >= 73728 ? 73728 : 0) : (rd_abs(U) >> 16) << 16; }
constexpr int rd_prevwin(int U) { for (int q = U - 1; q >= 0; --q) if (PG.u[q].base == PG.u[U].base) return rd_win(q); return 0; }
constexpr int rd_h(int U) { return PG.u[U].kind == K_G ? 4608 : ((PG.u[U].base < 4 || PG.u[U].base >= 12) ? 2048 : 4096); }
constexpr int dma_in(int U, int t) { if (U >= PG.nu) return -1; for (int i = 0; i < PG.nm[U][t]; ++i) if (PG.m[U][t][i].k <= V_LASTDMA) return i; return -1; }

__global__ __launch_bounds__(256, 1) void kf(const char* __restrict__ x, const char* __restrict__ wqkv, const char* __restrict__ wo, const char* __restrict__ wg,
                                              const char* __restrict__ wu, const char* __restrict__ wd, const char* __restrict__ gw, char* __restrict__ ws, char* __restrict__ out) {
  extern __shared__ __attribute__((aligned(16))) char smem[];
  const int tid = threadIdx.x; const int lane0 = tid & 63;
  const int wave = __builtin_amdgcn_readfirstlane(tid >> 6);
  const int bid = blockIdx.x, xcd = bid & 7, idx = bid >> 3;
  const int m = 4 * (xcd & 1) + (idx & 3), c = 8 * (xcd >> 1) + (idx >> 2), gam = c >> 2, ci = c & 3;
  const int s = xcd >> 1, mt = 8 * (xcd & 1) + (idx & 7), nt = idx >> 3;
  const int tg2 = wave & 1, kh = wave >> 1;
  const unsigned lbase = (unsigned)(size_t)smem;
  asm volatile("" : "+s"(x), "+s"(wqkv), "+s"(wo), "+s"(wg), "+s"(wu), "+s"(wd), "+s"(gw), "+s"(ws), "+s"(out));
  {
    u64 pc; unsigned csz;
    asm volatile("s_getpc_b64 %0\n.Lkf_code_begin:\n\ts_mov_b32 %1, .Lkf_code_end-.Lkf_code_begin" : "=s"(pc), "=s"(csz));
    setm0<PFOFF>(lbase); NOP0();
    touch(((lane0 & 7) * 128 + idx * 4 + wave) * 128, mksrd((const void*)pc, csz), 0u);
  }
  const unsigned vx = (lane0 >> 3) * 1152 + (((lane0 & 7) ^ (lane0 >> 3)) << 4);
  unsigned vw[2];
  vw[0] = kap(8 * wave + (lane0 >> 3)) * 9216 + (((((lane0 >> 1) & 3) ^ (lane0 >> 4)) << 1 | (lane0 & 1)) << 4); vw[1] = vw[0] + 32 * 9216;
  const unsigned wcol = 1152 * gam + 256 * ci;
  const u32x4 rx = mksrd(x + (size_t)(256 * m + 64 * wave) * 1152, 64 * 1152);
  const u32x4 rg = mksrd(wg + wcol, 576 * 9216 - wcol), ru = mksrd(wu + wcol, 576 * 9216 - wcol);
  const unsigned lw = lbase + 1024 * wave, lx = lbase + 9 * 8192 + 16384 * wave;
  const unsigned rot = (2 * (m & 3) + (m >> 2) + (c & 7)) % 9;
  u64 kd = (0x876543210ull >> (4 * rot)) | (0x876543210ull << (36 - 4 * rot));
  asm volatile("" : "+s"(kd));
  auto kdd = [&](int d) -> unsigned { return (unsigned)(kd >> (4 * d)) & 15u; };
  unsigned vh = lane0 * 16; PIN(vh);
  unsigned sow = 0, sox = 0, lhx = 0;
  const char* pq = nullptr; const char* phx = nullptr; const char* pv64 = nullptr; const char* ph64 = nullptr; unsigned pqs = 0;
  unsigned lg = 0, vg[5], vpf[2], vh2[2], vk2[2], vpl8, vplk, vks, vfl, vlo[2], vlb, obase; u32x2 fl, lf[2]; f32x2 zv; u32x4 kt[5], rk;
  u32x4 rd, ro, rh, rf; unsigned jt[4], hbase, sbase;
  bf16x8 xf[18][4], hr[NG][4]; u32x2 pl[NLEV + 1];
  unsigned tlo = 0, thi = 0;
  auto jget = [&](int i) -> unsigned { return ((jt[i < 8 ? i >> 2 : (i < 16 ? 2 : 3)] >> (8 * (i & 3))) & 255u) + (i >= 12 && i < 16 ? 1u : 0u); };
  auto prep = [&](auto KK, auto AA) {
    constexpr int k = decltype(KK)::v, a = decltype(AA)::v;
    if constexpr (k == V_WB) {
      constexpr int b = a >> 1;
      if constexpr (b < 36) { sow = kdd(b < 18 ? b >> 1 : (b - 18) % 9) * (64 * 9216) + (b < 18 ? 0 : 128); asm volatile("" : "+s"(sow)); }
      else { sow = kdd((b - 36) >> 1) * 16384 + (b & 1) * 8192; asm volatile("" : "+s"(sow)); }
    }
    if constexpr (k == V_XP) {
      if constexpr ((a & 7) == 0) sox = kdd(a >> 3) * 128; else sox += 8 * 1152;
      asm volatile("" : "+s"(sox)); }
    if constexpr (k == V_HX) {
      unsigned pc = wave + 4 * a; if (a == 4 && pc >= 18) pc -= 4;
      lhx = lbase + pc * 1024; ph64 = phx + 128 * ((unsigned)(kd >> (4 * (pc >> 1))) & 15u); PIN(ph64); asm volatile("" : "+s"(lhx));
    }
    if constexpr (k == V_KL) { pv64 = pq + (size_t)pqs * (unsigned)((9 * ci) / 2 + a); PIN(pv64); }
    if constexpr (k == V_KS) { sox = ((9 * ci) / 2 + a) * 8192; asm volatile("" : "+s"(sox)); }
    if constexpr (k == V_PF) { sox = a < 6 ? a * 2048 : 0; asm volatile("" : "+s"(sox)); }
    if constexpr (k == V_GW) { if constexpr ((a >> 3) < 18) sow = jget(a >> 3) * (32 * 1152); else sow = ((jt[3] >> ((a >> 3) == 18 ? 1 : 9)) & 1u) * (72 * 1152); asm volatile("" : "+s"(sow)); }
    if constexpr (k == V_GH) { if constexpr ((a >> 2) < 18) sox = hbase + jget(a >> 2) * 4096; else sox = obase + ((jt[3] >> ((a >> 2) == 18 ? 1 : 9)) & 1u) * 8192; asm volatile("" : "+s"(sox)); }
    if constexpr (k == V_FL) { fl[0] = tlo + a; PIN(fl); }
  };
  auto m0for = [&](auto KK, auto AA) {
    constexpr int k = decltype(KK)::v, a = decltype(AA)::v;
    if constexpr (k == V_WB) setm0<PG.boff[a >> 1] + (a & 1) * 4096>(lw);
    if constexpr (k == V_XP) setm0<((a >> 3) & 1) * 8192 + 1024 * (a & 7)>(lx);
    if constexpr (k == V_HX) setm0<HXOFF>(lhx);
    if constexpr (k == V_GW) setm0<PG.boff[NB1 + (a >> 3)] + 1024 * (a & 7)>(lg);
    if constexpr (k == V_OW) { if constexpr (a == 0) setm0<LOFF>(lw); else setm0<LOFF + 4096>(lbase); }
    if constexpr (k == V_PF) setm0<PFOFF>(lbase);
  };
  auto issue = [&](auto KK, auto AA) {
    constexpr int k = decltype(KK)::v, a = decltype(AA)::v;
    if constexpr (k == V_WB) {
      constexpr int b = a >> 1;
      if constexpr (b < 18) bdma<0>(vw[a & 1], (b & 1) ? ru : rg, sow);
      else if constexpr (b < 36) bdma<0>(vw[a & 1], b < 27 ? rg : ru, sow);
      else bdmac(vk2[a & 1], rk, sow);
    }
    if constexpr (k == V_XP) bdma<0>(vx, rx, sox);
    if constexpr (k == V_HX) vdma(ph64);
    if constexpr (k == V_GW) { if constexpr ((a & 7) < 4) bdma<0>(vg[a & 7], (a >> 3) < 18 ? rd : ro, sow); else bdma32(vg[4], (a >> 3) < 18 ? rd : ro, sow); }
    if constexpr (k == V_OW) {
      const u32x4 rl = mksrd(wo + (144 * s + 16) * 1152 + 288 * nt, 128 * 1152);
      if constexpr (a == 0) bdma<0>(vlo[0], rl, 0u); else bdma32(vlo[1], rl, 0u);
    }
    if constexpr (k == V_PF) {
      if constexpr (a < 6) touch(vpf[0], mksrd(wd + (size_t)s * (1152 * 1152), 1152 * 1152), sox); else touch(vpf[1], mksrd(wo + s * (144 * 1152), 144 * 1152), sox);
    }
    if constexpr (k == V_LB) {
      if constexpr (a < 2) ld8s<(a & 1) * 256>(lf[a], vlb, rh, (unsigned)((2 * mt + tg2) * GS + 160 * 4096 + 2048 * s + 128 * kh));
      else ld8s<0>(zv, vh, mksrd(ws + WS_Z + (s * 32 + 2 * mt + (s >> 1)) * 1024 + 8 * (s & 1), 1024), 0u);
    }
    if constexpr (k == V_KL) asm volatile("global_load_dwordx4 %0, %1, off" : "=v"(kt[a]) : "v"(pv64));
    if constexpr (k == V_KS) st16n<0>(vks, mksrd(ws + WS_PK + (size_t)gam * (4 * 147456), 4 * 147456), sox, kt[a]);
    if constexpr (k == V_GH) {
      constexpr int i = a >> 2, q = a & 3;
      if constexpr (i < 16) ld16a<AH(i, q), (q & 1) * 1024>(vh2[q >> 1], rh, sox); else ld16s<(q & 1) * 1024>(hr[i][q], vh2[q >> 1], rh, sox);
    }
    if constexpr (k == V_PL) ld8s<0>(pl[a], a == 1 ? vplk : vpl8, rf, 0u);
    if constexpr (k == V_FL) st8s(vfl, rf, fl);
  };
  auto now = [&](auto KK, auto AA, bool doprep) { if (doprep) prep(KK, AA); m0for(KK, AA); NOP0(); issue(KK, AA); };
  now(IC<V_WB>{}, IC<0>{}, true); now(IC<V_WB>{}, IC<2>{}, false);
  sfor<4>([&](auto A) { now(IC<V_XP>{}, A, true); });
  u64 epoch; asm volatile("s_load_dwordx2 %0, %1, 0x0" : "=s"(epoch) : "s"(ws + WS_FLAG + 16384));
  if (tid < 64) *(LAS unsigned*)(lbase + CTL + 4 * lane0) = 0u;
  unsigned axr[2]; axr[0] = lx + (lane0 & 15) * 128 + (((lane0 >> 4) ^ (lane0 & 7)) << 4); axr[1] = axr[0] ^ 64; PIN(axr[0]); PIN(axr[1]);
  asm volatile("" ::: "a0", "a255");
  unsigned acnt = lbase + CTL + 4 * lane0, one = 1u; PIN(acnt); PIN(one);
  unsigned bs[16], ahx, agw, av, avw, avr, avl, pmask, vo2;
  u64 mg0, mrow0;
  {
    const int l2 = lane0, tau = l2 & 15, g = l2 >> 4, q4 = tau >> 2, p4 = l2 & 3;
    {
      const int r = 8 * (wave & 1) + (l2 >> 3); int tok = 256 * m - 16 + r; if (tok < 0) tok = 0;
      phx = (r == 0 ? gw : x + (size_t)tok * 1152) + (((l2 & 7) ^ (l2 >> 3)) << 4); PIN(phx);
    }
    const unsigned aw0 = lbase + (4 * g + q4) * 128 + (((2 * g + (q4 >> 1)) & 3) << 5) + 8 * p4;
#pragma unroll
    for (int i = 0; i < 4; ++i) { bs[i] = aw0 ^ (i << 5); bs[12 + i] = bs[i]; }
    {
      const int rho = 4 * m + wave, uq = (tau >> 1) ^ (rho & 7), ch = (uq << 1) | (tau & 1), kr = kap(rho);
      const bool gu = ch >= 6 && ch < 10;
      const int seg = ch < 6 ? ch >> 1 : (ch < 13 ? ch - 10 : 2);
      pqs = gu ? 32 * 9216 : 32 * 3456;
      pq = (gu ? (ch < 8 ? wg : wu) : wqkv) + (kr * (pqs >> 5) + (gu ? 1152 * gam + 1024 + 32 * g + 16 * (ch & 1) : 1152 * seg + 36 * (4 * gam + g) + (ch < 6 ? 16 * (ch & 1) : 20)));
      vks = g * 147456 + rho * 256 + tau * 16; PIN(vks);
      PIN(pq); PIN(pqs);
    }
    const int rr = 4 * g + q4;
    const unsigned aq0 = lbase + rr * 256 + ((rr & 7) << 5) + 8 * p4;
    const unsigned aq3 = lbase + rr * 256 + (((p4 < 2 ? 5 : 6) ^ (rr & 7)) << 5) + ((p4 & 1) ? 24 : 8);
    bs[4] = aq0; bs[5] = aq0 ^ 32; bs[6] = aq0 ^ 64; bs[7] = aq3; bs[8] = aq0 ^ 96; bs[9] = aq0 ^ 128;
    bs[10] = wave == 0 ? bs[5] : (wave == 1 ? bs[6] : aq3);
    bs[11] = lbase + kh * 9216 + rr * 288 + 8 * p4;
#pragma unroll
    for (int i = 0; i < 16; ++i) PIN(bs[i]);
    ahx = lbase + HXOFF + tau * 128 + ((g ^ (l2 & 7)) << 4); PIN(ahx); agw = lbase + HXOFF + g * 16; PIN(agw);
#pragma unroll
    for (int i = 0; i < 5; ++i) { const int q = 64 * i + (i == 4 ? (l2 & 31) : l2); vg[i] = (q / 18) * 1152 + (q % 18) * 16; PIN(vg[i]); }
    vpf[0] = ((idx * 4 + wave) * 81 + (l2 >> 2)) * 128; vpf[1] = ((idx * 4 + wave) * 11 + (l2 >> 2)) * 128;
    vk2[0] = vh + 1024 * wave; vk2[1] = vk2[0] + 4096; vplk = (((l2 >> 2) & 7) * 32 + 4 * gam + (l2 & 3)) * 8;
    rk = mksrd(ws + WS_PK + (size_t)c * 147456, 147456);
    vpl8 = ((mt >> 1) * 32 + 8 * s + (l2 & 7)) * 8;
    { const int q0 = 64 * wave + l2, q1 = 256 + (l2 & 31);
      vlo[0] = (18 * (q0 / 36) + (q0 / 18 & 1)) * 1152 + (q0 % 18) * 16; vlo[1] = (18 * (q1 / 36) + (q1 / 18 & 1)) * 1152 + (q1 % 18) * 16; PIN(vlo[0]); PIN(vlo[1]); }
    vfl = tid == 0 ? (unsigned)(m * 32 + c) * 8 : 0x7fffffffu;
    vh2[0] = vh + kh * 2048; vh2[1] = vh + (1 - kh) * 2048;
    PIN(vpf[0]); PIN(vpf[1]); PIN(vk2[0]); PIN(vk2[1]); PIN(vplk); PIN(vpl8); PIN(vfl); PIN(vh2[0]); PIN(vh2[1]);
    mg0 = 0xffffull; mrow0 = 0x1111111111111111ull;
    {
      u32x4 pk;
      if (wave < 2) {
#pragma unroll
        for (int e = 0; e < 4; ++e) { const int d = tau + 16 - 16 * wave - (4 * g + e); pk[e] = ((unsigned)d <= 14u && !(d & 1)) ? 0u : 0xff61b1e6u; }
      } else if (wave == 2) {
        const unsigned vrow = lbase + HXOFF + (16 + tau) * AVS, rrow = lbase + HXOFF + rr * AVS;
        pk = (u32x4){vrow + 8 * g, vrow + (g == 2 ? 32u : 40u), rrow + 8 * p4, rrow + 32};
      } else {
        pk = (u32x4){(tau & 1) ? 0xffff0000u : 0xffffu, (unsigned)((g >> 1) * 128 + tau * 8 + (g & 1) * 4), (unsigned)(g * 512 + tau * 8), 0x3f803f80u};
        *(LAS u32x4*)(lbase + PARK + 4096 + vh) = (u32x4){0xff61b1e6u, 0xff61b1e6u, 0xff61b1e6u, 0xff61b1e6u};
      }
      *(LAS u32x4*)(lbase + PARK + vk2[0]) = pk;
    }
    const unsigned od = (unsigned)(36 * s + kh) * (32 * 1152) + 16 * tg2 * 1152 + 288 * nt, oo = (unsigned)(144 * s + 36 * kh + 18 * tg2) * 1152 + 288 * nt;
    rd = mksrd(wd + od, 4608 * 1152 - od); ro = mksrd(wo + oo, 576 * 1152 - oo);
    rh = mksrd(ws + WS_HF, HF_BYTES); rf = mksrd(ws + WS_FLAG, 65536);
    lg = lbase + kh * 9216 + tg2 * 4608; obase = (2 * mt + tg2) * GS + (144 + 4 * s + kh) * 4096;
    hbase = (2 * mt + tg2) * GS + (36 * s + kh) * 4096; sbase = (4 * m + wave) * GS;
    const unsigned rot2 = 2 * nt + mt;
    {
      const u64 ta = 0x1e1a16120c080400ull; const unsigned ra = 8 * (rot2 & 7), tb = 0x1c140a02u + kh * 0x03030303u, rb = 8 * (rot2 & 3);
      const u64 ja = (ta >> ra) | ((ta << 1) << (63 - ra));
      jt[0] = (unsigned)ja; jt[1] = (unsigned)(ja >> 32);
      jt[2] = (unsigned)((((u64)tb << 32) | tb) >> rb);
      jt[3] = (rot2 & 1) ? 0x1022u : 0x2210u;
    }
    asm volatile("" : "+s"(rd), "+s"(ro), "+s"(rh), "+s"(rf), "+s"(wo), "+s"(wd), "+s"(rk), "+s"(out), "+s"(lg), "+s"(obase), "+s"(hbase), "+s"(sbase), "+s"(jt[0]), "+s"(jt[1]), "+s"(jt[2]), "+s"(jt[3]));
  }
  unsigned sline = sbase + (18 * gam + 4 * ci) * 4096; asm volatile("" : "+s"(sline));
  bf16x8 xh, gwf; u32x4 asel;
  f32x4 acc[16][4], acch, ga[9][4];
  u32x4 hq[4][8];
  const float nl2e = -LOG2E, SC = 0.23570226039551584f * LOG2E, NSC = -SC;

  auto whalf = [&](auto UU, int h) -> bf16x4 {
    constexpr int U = decltype(UU)::v, bi = PG.u[U].base, win = rd_win(U), pw = rd_prevwin(U);
    if (h == 0 && win != pw) { bs[bi] += (unsigned)(win - pw); PIN(bs[bi]); }
    return trhalf(bs[bi], rd_abs(U) - win + h * rd_h(U));
  };
  auto check = [&](auto PP) {
    constexpr int p = decltype(PP)::v;
    chkflag<(PG.chkw[p] > 63 ? 63 : PG.chkw[p])>(pl[p], p == 1 ? vplk : vpl8, rf, ((u64)thi << 32) | (tlo + p), ((u64)thi << 32) | (tlo + 7));
  };
  u32x4 qf[4], kf[4], kp, pw[4], ow[2]; u32x2 vv[4], lw2[4]; bf16x4 vt[4][4]; f32x4 s0[4], s1[4], o0[4], o1[4], cm0, cm1, cmf;
  float cv[4][4], mx[4], mt2[4], om[4][6], ez[4]; unsigned lq[4], ol = 0, lv[4], ones = 0, akx = 0, ha = 0, hb = 0, ht = 0, apk = 0; u32x4 p0, p1; u32x2 hp, hq2;

  SB();
  sfor<4>([&](auto A) { now(IC<V_XP>{}, IC<4 + A.v>{}, true); });
  now(IC<V_WB>{}, IC<1>{}, false); now(IC<V_WB>{}, IC<3>{}, false);
  sfor<8>([&](auto A) { now(IC<V_XP>{}, IC<8 + A.v>{}, true); });
  waitv<PG.nfirst>();
  asm volatile("s_waitcnt lgkmcnt(0)" : "+s"(epoch) :: "memory");
  BAR();
  sfor<8>([&](auto I) { lds_to_a<AX(I.v >> 2, I.v & 3), (I.v & 3) * 2048>(axr[I.v >> 2]); });
  bf16x4 w[PG.nu + PD + 1][2];
  sfor<PD>([&](auto J) { w[J.v][0] = whalf(J, 0); w[J.v][1] = whalf(J, 1); });
  unsigned pv = 0;
  {
    u64 tag = epoch;
    tag = (tag ^ (tag >> 29)) * 0x9E3779B97F4A7C15ull + 0x632BE59BD9B4E019ull; tag ^= tag >> 32; tag *= 0xD6E8FEB86659FD93ull; tag &= ~7ull;
    tlo = (unsigned)tag; thi = (unsigned)(tag >> 32);
    asm volatile("" : "+s"(tlo), "+s"(thi));
    fl[0] = 0; fl[1] = thi; PIN(fl);
  }

  auto mop = [&](auto KK, auto AA) {
    constexpr int k = decltype(KK)::v, a = decltype(AA)::v;
    if constexpr (k < V_ST) issue(KK, AA);
    if constexpr (k == P_SO) prep(IC<a / 1024>{}, IC<a % 1024>{});
    if constexpr (k == S_ARR) { waitv<PG.nwait[a]>(); asm volatile("ds_add_u32 %0, %1" ::"v"(acnt), "v"(one) : "memory"); }
    if constexpr (k == S_KW) asm volatile("s_waitcnt vmcnt(%5)" : "+v"(kt[0]), "+v"(kt[1]), "+v"(kt[2]), "+v"(kt[3]), "+v"(kt[4]) : "n"(PG.kwait > 63 ? 63 : PG.kwait) : "memory");
    if constexpr (k == S_XW) waitv<PG.xwait[a]>();
    if constexpr (k == S_PV) pv = *(volatile LAS unsigned*)(acnt);
    if constexpr (k == S_CMP) asm volatile("v_cmp_gt_u32 vcc, %1, %0" ::"v"(pv), "i"(4 * (a + 1)) : "vcc");
    if constexpr (k == S_BR)
      asm volatile("s_cbranch_vccnz .Lkf_slow%=\n.Lkf_back%=:\n\t.subsection 1\n.Lkf_slow%=:\n\tds_read_b32 %0, %1\n\ts_waitcnt lgkmcnt(0)\n\tv_cmp_gt_u32 vcc, %2, %0\n\t"
                   "s_cbranch_vccnz .Lkf_slow%=\n\ts_branch .Lkf_back%=\n\t.subsection 0" : "+v"(pv) : "v"(acnt), "i"(4 * (a + 1)) : "vcc", "memory");
    if constexpr (k == F_XR) { constexpr int n = a >> 3, i = a & 7; constexpr int ks = 2 * n + 2 + (i & 1), O = ((n + 1) & 1) * 8192 + (i >> 1) * 2048;
      if constexpr (ks < 16) lds_to_a<AX(ks, i >> 1), O>(axr[i & 1]); else xf[ks][i >> 1] = ld128(axr[i & 1], O); }
    if constexpr (k == F_XH && a < 32) gwf = ld128(agw, (a >> 1) * 2048 + (a & 1) * 64);
    if constexpr (k == F_XH && a >= 32) xh = ld128(ahx ^ ((a & 1) << 6), ((a - 32) >> 1) * 2048);
    if constexpr (k == F_SEL) {
      constexpr int U = a >> 1, h = a & 1;
      const u32x2 wv = __builtin_bit_cast(u32x2, w[U][h]); const u32x4 hv = __builtin_bit_cast(u32x4, gwf);
      asel[2 * h] = vsel(mrow0, hv[2 * h], wv[0]); asel[2 * h + 1] = vsel(mrow0, hv[2 * h + 1], wv[1]);
    }
    if constexpr (k == F_FIX) fix(hr[a >> 1][2 * (a & 1)], hr[a >> 1][2 * (a & 1) + 1]);
    if constexpr (k == F_CHK) check(AA);
    if constexpr (k == F_E) {
      constexpr int o = a & 31, sI = (a >> 5) & 1, grp = (a >> 6) & 15, id = a >> 10;
      constexpr int gs = id == T_EA ? 4 * (grp >> 2) + 2 * sI : (id == T_EQ ? 4 : 8 + 2 * (grp >> 2) + sI), us = id == T_EA ? gs + 1 : (id == T_EQ ? 5 : gs + 4);
      constexpr int t2 = id == T_EQ ? 2 * grp + sI : grp & 3, i = o & 3;
      f32x4& gq = acc[gs][t2];
      if constexpr (o < 4) gq[i] = vmuls(nl2e, gq[i]);
      else if constexpr (o < 8) gq[i] = vexp(gq[i]);
      else if constexpr (o < 12) gq[i] = vadd1(gq[i]);
      else if constexpr (o < 16) gq[i] = vrcp(gq[i]);
      else if constexpr (o < 20) gq[i] = vmul(gq[i], acc[us][t2][i]);
      else hq[id][grp][2 * sI + (o & 1)] = vcvt(gq[2 * (o & 1)], gq[2 * (o & 1) + 1]);
    }
    if constexpr (k == V_ST) {
      constexpr int id = a >> 6, grp = a & 63;
      if constexpr (id == T_EA || id == T_EB) st16n<(grp & 3) * 1024>(vh, rh, sline + ((id == T_EB ? 2 : 0) + (grp >> 2)) * 4096, hq[id][grp]);
      if constexpr (id == T_EQ) st16n<grp * 2048>(vh, rh, sbase + (18 * gam + 16 + (ci >> 1)) * 4096 + 1024 * (ci & 1), hq[id][grp]);
      if constexpr (id == 8 && grp < 2) st16n<grp * 2048>(vh, rh, sbase + (144 + (c >> 1)) * 4096 + 1024 * (c & 1), ow[grp]);
      if constexpr (id == 8 && grp == 2) st4s(vo2, rh, sbase + 160 * 4096 + 256 * c, ol);
      if constexpr (id == 9)
        st16n<0>(vh, mksrd(ws + WS_Z + ((c >> 3) * 32 + 4 * m + wave) * 1024, (c & 7) == 0 ? 1024u : 0u), 0u,
                 (u32x4){__float_as_uint(acc[3][0][0]), __float_as_uint(acc[3][1][0]), __float_as_uint(acc[3][2][0]), __float_as_uint(acc[3][3][0])});
    }
    if constexpr (k == F_ATT && a >= 1100) {
      constexpr int o = a - 1100;
      if constexpr (o == 0) p0 = *(LAS u32x4*)(apk + 2048);
      if constexpr (o == 1) p1 = *(LAS u32x4*)(apk + 3072);
      if constexpr (o == 2) cm0 = *(LAS f32x4*)(apk);
      if constexpr (o == 3) cm1 = *(LAS f32x4*)(apk + 1024);
      if constexpr (o == 4) cmf = *(LAS f32x4*)(apk + ((m == 0 && wave == 0) ? 4096 : 0));
      if constexpr (o == 5) av = vadds(wave * (64 * AVS), p0[0]);
      if constexpr (o == 6) avw = vadds(wave * (64 * AVS), p0[1]);
      if constexpr (o == 7) avr = vadds(wave * (64 * AVS), p0[2]);
      if constexpr (o == 8) { avl = vadds(wave * (64 * AVS), p0[3]); pmask = p1[0]; vo2 = p1[1]; vlb = p1[2]; ones = p1[3]; }
      if constexpr (o == 9) akx = vadds(lbase + AKX + wave * 1024, vh);
      if constexpr (o == 10) hp[0] = vcvt(acch[0], acch[1]);
      if constexpr (o == 11) hp[1] = vcvt(acch[2], acch[3]);
      if constexpr (o == 12) ha = vsel(smask<1, true>(wave), p0[0] , vh);
      if constexpr (o == 13) hq2[0] = vmov(hp[1]);
      if constexpr (o == 14) ha = vadds(wave == 1 ? (unsigned)(-16 * AVS) : lbase + AKX + (wave == 2 ? 8u : (wave == 3 ? 4096u : 0u)), ha);
      if constexpr (o == 15) { hq2[1] = vmov(ones); ht = vmov(hp[1]); }
      if constexpr (o == 16) hb = vsel(smask<2, true>(wave), p0[1], vh);
      if constexpr (o == 17) { unsigned t = vmov(hp[1]); asm volatile("s_nop 0"); vswap16(t, ht); }
      if constexpr (o == 18) hb = vadds(wave == 2 ? (unsigned)(-16 * AVS) : lbase + AKX + 4096u, hb);
      if constexpr (o == 19) hp[0] = vsel(smask<2, true>(wave), ht, hp[0]);
      if constexpr (o == 20) hp[1] = vsel0(smask<2, false>(wave), hp[1]);
      if constexpr (o == 21) *(LAS u32x4*)(akx + 1024) = kf[3];
      if constexpr (o == 22) *(LAS u32x2*)(ha) = hp;
      if constexpr (o == 23) *(LAS u32x2*)(hb) = hq2;
    }
    if constexpr (k == F_ATT && a == 1099) apk = vadds(lbase + PARK, vh);
    if constexpr (k == F_ATT && a == 1001) kp = *(LAS u32x4*)(akx);
    if constexpr (k == F_ATT && a < 1000) {
      constexpr int r = a >> 2, q = a & 3;
      if constexpr (r == 0 || r == 1) kf[q][r] = vcvt(acc[1][q][2 * r], acc[1][q][2 * r + 1]);
      if constexpr (r == 2) { PIN(acc[3][q]); lw2[q][0] = vcvt(acc[3][q][2], acc[3][q][3]); }
      if constexpr (r == 3 || r == 4) vv[q][r - 3] = vcvt(acc[2][q][2 * (r - 3)], acc[2][q][2 * (r - 3) + 1]);
      if constexpr (r == 5) { *(LAS u32x2*)(av + q * 16 * AVS) = vv[q]; lw2[q][1] = vmov(ones); }
      if constexpr (r == 6) { *(LAS u32x2*)(avw + q * 16 * AVS) = lw2[q]; lq[q] = vmov(lw2[q][0]); }
      if constexpr (r == 7) kf[q][2] = vmov(lw2[q][0]);
      if constexpr (r == 8) { unsigned t = kf[q][2]; vswap16(lq[q], t); kf[q][2] = t; }
      if constexpr (r == 9) kf[q][3] = vzero();
      if constexpr (r == 10 || r == 11) qf[q][r - 10] = vcvt(acc[0][q][2 * (r - 10)], acc[0][q][2 * (r - 10) + 1]);
      if constexpr (r == 12) qf[q][2] = vsel0(mg0, lq[q]);
      if constexpr (r == 13) qf[q][3] = vzero();
      if constexpr (r == 14) mmac(s1[q], kf[q], qf[q], cm1);
      if constexpr (r == 15) mmac(s0[q], q == 0 ? kp : kf[q == 0 ? 0 : q - 1], qf[q], q == 0 ? cmf : cm0);
      if constexpr (r >= 16 && r < 20) { constexpr int i = (r - 16) ^ 2; const f32x4& sq = i < 2 ? s0[q] : s1[q]; cv[q][i] = vmax(sq[2 * (i & 1)], sq[2 * (i & 1) + 1]); }
      if constexpr (r == 20) mx[q] = vmax3(cv[q][2], cv[q][3], cv[q][0]);
      if constexpr (r == 21) mx[q] = vmax(mx[q], cv[q][1]);
      if constexpr (r == 22 || r == 25) mt2[q] = vmov(mx[q]);
      if constexpr (r == 23) vswap16(mx[q], mt2[q]);
      if constexpr (r == 26) vswap32(mx[q], mt2[q]);
      if constexpr (r == 24 || r == 27) mx[q] = vmax(mx[q], mt2[q]);
      if constexpr (r == 28) mx[q] = vmuls(NSC, mx[q]);
      if constexpr (r >= 29 && r < 33) cv[q][r - 29] = vfmas(cv[q][r - 29], SC, mx[q]);
      if constexpr (r >= 33 && r < 37) cv[q][r - 33] = vexp(cv[q][r - 33]);
      if constexpr (r >= 37 && r < 41) vt[q][r - 37] = trhalf(r < 39 ? avr : avl, (q + ((r - 37) & 1)) * 16 * AVS);
      if constexpr (r >= 41 && r < 45) pw[q][r - 41] = vcvt(cv[q][r - 41], cv[q][r - 41]);
      if constexpr (r >= 45 && r < 49) pw[q][r - 45] = vand(pw[q][r - 45], pmask);
      if constexpr (r == 49) mma<true>(o1[q], cat(vt[q][2], vt[q][3]), __builtin_bit_cast(bf16x8, pw[q]));
      if constexpr (r == 50) mma<true>(o0[q], cat(vt[q][0], vt[q][1]), __builtin_bit_cast(bf16x8, pw[q]));
      if constexpr (r == 51) { PIN(o1[q]); mx[q] = vrcp(o1[q][2]); }
      if constexpr (r == 62) ez[q] = vmins(ZMAX, acc[3][q][0]);
      if constexpr (r == 63) ez[q] = vmuls(LOG2E, ez[q]);
      if constexpr (r == 64) ez[q] = vexp(ez[q]);
      if constexpr (r == 65) mx[q] = vmul(mx[q], ez[q]);
      if constexpr (r == 52 || r == 53) om[q][r - 48] = vmul(o1[q][r - 52], mx[q]);
      if constexpr (r >= 54 && r < 58) om[q][r - 54] = vmul(o0[q][r - 54], mx[q]);
      if constexpr (r == 58) lv[q] = vcvt(om[q][4], om[q][5]);
      if constexpr (r == 59 || r == 60) ow[q >> 1][2 * (q & 1) + r - 59] = vcvt(om[q][2 * (r - 59)], om[q][2 * (r - 59) + 1]);
      if constexpr (r == 61) { if constexpr (q == 0) ol = lv[0]; else ol = vsel(0xffffull << (16 * q), lv[q], ol); }
    }
  };

  sfor<PG.nu>([&](auto UU) {
    constexpr int U = decltype(UU)::v; constexpr Unit un = PG.u[U];
    constexpr int n = un.stage, lu = U - PG.sfirst[n];
    SB();
    bf16x8 a;
    if constexpr (un.kind == K_SEL) a = __builtin_bit_cast(bf16x8, asel); else a = cat(w[U][0], w[U][1]);
    sfor<ngap(PG, U)>([&](auto T) {
      constexpr int t = decltype(T)::v;
      if constexpr (un.kind == K_HALO) mma<un.ks == 0>(acch, a, xh);
      else if constexpr (un.kind == K_G && un.ks < 16) { if constexpr (t < 2) mma_va<AH(un.ks, t), un.ks == 0>(ga[un.slot][t], a); else mma_aa<AG(un.slot, t), AH(un.ks, t), un.ks == 0>(a); }
      else if constexpr (un.kind == K_G) { if constexpr (t < 2) mma<false>(ga[un.slot][t], a, hr[un.ks][t]); else mma_av<AG(un.slot, t)>(a, hr[un.ks][t]); }
      else if constexpr (n < 15) {
        constexpr int U0 = U & ~1, mI = 4 * (U & 1) + t, UQ = U0 + (mI & 1), tq = mI >> 1, kq = PG.u[UQ].ks;
        const bf16x8 aq = cat(w[UQ][0], w[UQ][1]);
        if constexpr (kq < 16) mma_va<AX(kq, tq), kq == 0>(acc[un.slot][tq], aq); else mma<false>(acc[un.slot][tq], aq, xf[kq][tq]);
      }
      else if constexpr (un.ks < 16) mma_va<AX(un.ks, t), un.ks == 0>(acc[un.slot][t], a);
      else mma<false>(acc[un.slot][t], a, xf[un.ks][t]);
      SB();
      constexpr int NU = t + 1 < ngap(PG, U) ? U : U + 1, NT = t + 1 < ngap(PG, U) ? t + 1 : 0;
      constexpr int nd = dma_in(NU, NT), od = dma_in(U, t);
      constexpr bool brfirst = PG.nm[U][t] > 0 && PG.m[U][t][0].k == S_BR;
      if constexpr (brfirst) { mop(IC<S_BR>{}, IC<PG.m[U][t][0].a>{}); SB(); }
      constexpr bool late = nd >= 0 && (od >= 0 || PG.m[NU][NT][nd >= 0 ? nd : 0].k == V_HX);
      if constexpr (nd >= 0 && !late) { m0for(IC<PG.m[NU][NT][nd >= 0 ? nd : 0].k>{}, IC<PG.m[NU][NT][nd >= 0 ? nd : 0].a>{}); SB(); }
      constexpr int UR = un.kind == K_SEL ? U + PD + (t >> 1) : U + PD;
      if constexpr (un.kind != K_HALO && (t >= 2 || un.kind == K_SEL) && UR < PG.nu) { w[UR][t & 1] = whalf(IC<(UR < PG.nu ? UR : 0)>{}, t & 1); SB(); }
      sfor<PG.nm[U][t]>([&](auto I) {
        if constexpr (!(brfirst && I.v == 0)) { mop(IC<PG.m[U][t][I.v].k>{}, IC<PG.m[U][t][I.v].a>{}); SB(); }
      });
      if constexpr (late) { m0for(IC<PG.m[NU][NT][nd >= 0 ? nd : 0].k>{}, IC<PG.m[NU][NT][nd >= 0 ? nd : 0].a>{}); SB(); }
    });
  });
  SB();
  const int lane = lane_id();
  const int grp = mt * 4 + nt, quarter = 2 * tg2 + kh;
  const unsigned xw = lbase + XOFF + wave * 18432, xr = lbase + XOFF + (wave ^ 2) * 18432;
  {
    const unsigned bl = bs[11] + (LOFF - rd_win(PG.nu - 1)) - kh * 9216;
    bf16x4 wl[9];
#pragma unroll
    for (int j = 0; j < 9; ++j) wl[j] = trhalf(bl, 32 * j);
    asm volatile("s_waitcnt vmcnt(0)" : "+v"(lf[0]), "+v"(lf[1]), "+v"(zv) :: "memory");
    { unsigned a = lf[0][1], b = lf[1][0]; asm volatile("v_swap_b32 %0, %1" : "+v"(a), "+v"(b)); lf[0][1] = a; lf[1][0] = b; }
    asm volatile("s_nop 1" : "+v"(lf[0]), "+v"(lf[1]));
    const unsigned xwa = xw + vh;
    SB();
    sfor<18>([&](auto J) { mma16(ga[J.v >> 1][J.v & 1], wl[J.v >> 1], lf[J.v & 1]); SB(); a_to_lds<AG(J.v >> 1, 2 + (J.v & 1)), J.v * 1024>(xwa); SB(); });
  }
  asm volatile("s_waitcnt lgkmcnt(0)" ::: "memory");
  BAR();
  const u32x4 rm = mksrd(ws + WS_MAIL + (size_t)grp * (16 * 9216), 16 * 9216);
  f32x4 xin[9][2];
#pragma unroll
  for (int j = 0; j < 9; ++j) { xin[j][0] = *(LAS f32x4*)(xr + vh + j * 2048); xin[j][1] = *(LAS f32x4*)(xr + vh + j * 2048 + 1024); }
  if (quarter != s) {
    const unsigned so = (quarter * 4 + s) * 9216;
    sfor<9>([&](auto J) { constexpr int j = J.v;
      const f32x4 a = ga[j][0] + xin[j][0], b = ga[j][1] + xin[j][1];
      st16n<(j & 3) * 1024>(vh, rm, so + (j >> 2) * 4096, (u32x4){pk2(a[0], a[1]), pk2(a[2], a[3]), pk2(b[0], b[1]), pk2(b[2], b[3])}); });
    asm volatile("s_waitcnt vmcnt(0)" ::: "memory");
    st8s(lane == 0 ? (unsigned)(RFO + (grp * 4 + quarter) * RFS + s * 8) : 0x7fffffffu, rf, (u32x2){tlo + 1u, thi});
  } else {
#pragma unroll
    for (int j = 0; j < 9; ++j) { *(LAS f32x4*)(xr + vh + j * 2048) = ga[j][0] + xin[j][0]; *(LAS f32x4*)(xr + vh + j * 2048 + 1024) = ga[j][1] + xin[j][1]; }
    if (bid == 0) {
      u32x2 ev[4];
      sfor<4>([&](auto I) { ld8s<I.v * 512>(ev[I.v], lane * 8, rf, 0u); });
      asm volatile("s_waitcnt vmcnt(0)" : "+v"(ev[0]), "+v"(ev[1]), "+v"(ev[2]), "+v"(ev[3]) :: "memory");
      sfor<4>([&](auto I) { chkflag<0>(ev[I.v], lane * 8 + I.v * 512, rf, ((u64)thi << 32) | (tlo + 1u), ((u64)thi << 32) | (tlo + 7u)); });
      st8s(lane == 0 ? 16384u : 0x7fffffffu, rf, (u32x2){tlo, thi});
    }
    {
      u32x2 rv = {0u, 0u};
      const int o3 = lane % 3;
      chkflag<63>(rv, RFO + (grp * 4 + s) * RFS + (o3 + (o3 >= s)) * 8, rf, ((u64)thi << 32) | (tlo + 1u), ((u64)thi << 32) | (tlo + 7u));
    }

  }
  asm volatile("s_waitcnt lgkmcnt(0)" ::: "memory");
  BAR();
  float og[2];
#pragma unroll
  for (int t = 0; t < 2; ++t) og[t] = __builtin_amdgcn_rcpf(1.f + __builtin_amdgcn_exp2f(fminf(zv[t], ZMAX) * LOG2E));
  f32x4 fb[3][2];
  const unsigned xo = lbase + XOFF + ((((s >> 1) | ((s & 1) << 1))) ^ 2) * 18432 + vh;
#pragma unroll
  for (int i = 0; i < 3; ++i) {
    const int j = wave + 4 * i < 9 ? wave + 4 * i : 8;
    fb[i][0] = *(LAS f32x4*)(xo + j * 2048); fb[i][1] = *(LAS f32x4*)(xo + j * 2048 + 1024);
  }
  {
    u32x4 mv[3][3];
    sfor<9>([&](auto Q) { constexpr int i = Q.v / 3, o = Q.v % 3;
      const int j = wave + 4 * i < 9 ? wave + 4 * i : 8;
      ld16s<0>(mv[i][o], vh, rm, (unsigned)((s * 4 + o + (o >= s)) * 9216 + j * 1024)); });
    char* op = out + (size_t)(128 * mt + 32 * s + (lane & 15)) * 1152 + (144 * nt + 16 * wave + 4 * (lane >> 4)) * 2;
    sfor<3>([&](auto I) { constexpr int i = I.v;
      asm volatile("s_waitcnt vmcnt(%3)" : "+v"(mv[i][0]), "+v"(mv[i][1]), "+v"(mv[i][2]) : "n"(6 - i) : "memory");
      if (i < 2 || wave == 0) {
#pragma unroll
        for (int t = 0; t < 2; ++t) {
          float r[4];
#pragma unroll
          for (int e = 0; e < 4; ++e) {
            float sum = fb[i][t][e];
#pragma unroll
            for (int o = 0; o < 3; ++o) { const unsigned d = mv[i][o][2 * t + (e >> 1)]; sum += __uint_as_float((e & 1) ? (d & 0xffff0000u) : (d << 16)); }
            r[e] = og[t] * sum;
          }
          stout<i * 128>(op + t * (16 * 1152), (u32x2){pk2(r[0], r[1]), pk2(r[2], r[3])});
        }
      }
    });
  }
  asm volatile(".subsection 2\n.Lkf_code_end:\n\t.fill 64, 4, 0xBF800000\n\t.subsection 0");
}

extern "C" void kernel_launch(void* const* d_in, const int* in_sizes, int n_in, void* d_out, int out_size, void* d_ws, size_t ws_size, hipStream_t stream) {
  (void)hipFuncSetAttribute((const void*)kf, hipFuncAttributeMaxDynamicSharedMemorySize, LDS_BYTES);
  hipLaunchKernelGGL(kf, dim3(256), dim3(256), LDS_BYTES, stream, (const char*)d_in[0], (const char*)d_in[1], (const char*)d_in[2], (const char*)d_in[4],
                     (const char*)d_in[5], (const char*)d_in[6], (const char*)d_in[7], (char*)d_ws, (char*)d_out);
}
```
